```python
import torch

def _softmax(x, axis=-1):
    return torch.softmax(x, dim=axis)
def _gelu(x):
    return torch.nn.functional.gelu(x, approximate="tanh")
def _silu(x):
    return torch.nn.functional.silu(x)
def _sigmoid(x):
    return torch.sigmoid(x)
def _layernorm(x, axis=-1, eps=1e-5):
    m = x.mean(dim=axis, keepdim=True)
    v = x.var(dim=axis, keepdim=True, unbiased=False)
    return (x - m) / torch.sqrt(v + eps)
def _scatter_add(tgt, idx, src, axis=0):
    return tgt.clone().index_add_(axis, idx, src)
def _segment_sum(src, seg, n):
    out = torch.zeros((n,) + src.shape[1:], dtype=src.dtype)
    return out.index_add_(0, seg, src)
def _pad(x, pw):
    flat = tuple(v for pair in reversed(pw) for v in pair)
    return torch.nn.functional.pad(x, flat)

kernel_name = 'recurrent_scan__compressed_kv_cla__dense_activation_gelu__decode'

def setup_inputs():

    _w = (lambda a: (lambda t: t.to(torch.bfloat16) if t.is_floating_point() else t)(torch.as_tensor(a)))
    def _A_setup():
        B, H, Sq, Sk, D, M = 2, 32, 1, 4096, 128, 512
        Dc = 4 * D
        return {
            "x": _w((lambda *s: torch.randn(*s, dtype=torch.bfloat16) * 0.1)(B, Sq, M)),
            "c_kv": _w((lambda *s: torch.randn(*s, dtype=torch.bfloat16) * 3.2)(B, Sk, Dc)),
            "w_q": _w((lambda *s: torch.randn(*s, dtype=torch.bfloat16) * 0.8)(M, H, D) * (M ** -0.5)),
            "w_uk": _w((lambda *s: torch.randn(*s, dtype=torch.bfloat16) * 0.8)(Dc, H, D) * (Dc ** -0.5)),
            "w_uv": _w((lambda *s: torch.randn(*s, dtype=torch.bfloat16) * 3.2)(Dc, H, D) * (Dc ** -0.5)),
            "w_o": _w((lambda *s: torch.randn(*s, dtype=torch.bfloat16) * 3.2)(H, D, M) * ((H * D) ** -0.5)),
        }
    _A_in = _A_setup()

    def _B_setup():
        B, S, D, F = 2, 1, 512, 1376
        return {
            "x": _w((lambda *s: torch.randn(*s, dtype=torch.bfloat16) * 0.1)(B, S, D)),
            "w1": _w((lambda *s: torch.randn(*s, dtype=torch.bfloat16) * 3.2)(D, F) * (D ** -0.5)),
            "b1": _w((lambda *s: torch.randn(*s, dtype=torch.bfloat16) * 3.2)(F) * 0.01),
            "w2": _w((lambda *s: torch.randn(*s, dtype=torch.bfloat16) * 3.2)(F, D) * (F ** -0.5)),
            "b2": _w((lambda *s: torch.randn(*s, dtype=torch.bfloat16) * 3.2)(D) * 0.01),
        }
    _B_in = _B_setup()

    return {
        "A_x": _A_in["x"],
        "A_c_kv": _A_in["c_kv"],
        "A_w_q": _A_in["w_q"],
        "A_w_uk": _A_in["w_uk"],
        "A_w_uv": _A_in["w_uv"],
        "A_w_o": _A_in["w_o"],
        "B_x": _B_in["x"],
        "B_w1": _B_in["w1"],
        "B_b1": _B_in["b1"],
        "B_w2": _B_in["w2"],
        "B_b2": _B_in["b2"],
    }

def reference(A_x, A_c_kv, A_w_q, A_w_uk, A_w_uv, A_w_o, B_x, B_w1, B_b1, B_w2, B_b2):
    def _A_ref(x, c_kv, w_q, w_uk, w_uv, w_o):
        B, Sq, M = x.shape[0], x.shape[1], x.shape[2]
        Sk = c_kv.shape[1]
        H, D = w_q.shape[1], w_q.shape[2]
        q = torch.einsum("bqm,mhd->bhqd", x, w_q)
        k = torch.einsum("bkc,chd->bhkd", c_kv, w_uk)
        v = torch.einsum("bkc,chd->bhkd", c_kv, w_uv)
        scores = torch.einsum("bhqd,bhkd->bhqk", q, k) * (D ** -0.5)
        off = Sk - Sq
        qi = torch.arange(Sq)[:, None]
        kj = torch.arange(Sk)[None, :]
        cmask = kj <= (qi + off)
        scores = torch.where(cmask[None, None], scores, -1e9)
        p = _softmax(scores, axis=-1)
        ctx = torch.einsum("bhqk,bhkd->bhqd", p, v)
        return torch.einsum("bhqd,hdm->bqm", ctx, w_o)

    def _B_ref(x, w1, b1, w2, b2):
        h = torch.einsum("bsd,df->bsf", x, w1) + b1
        h = _gelu(h)
        return torch.einsum("bsf,fd->bsd", h, w2) + b2

    x = A_x
    xn = _layernorm(x, axis=-1)
    s = _A_ref(xn, A_c_kv, A_w_q, A_w_uk, A_w_uv, A_w_o)
    g = _silu(_B_ref(xn, B_w1, B_b1, B_w2, B_b2))
    return x + s * g
```

```cpp
#include <hip/hip_runtime.h>
#include <hip/hip_bf16.h>
#include <stdint.h>

typedef unsigned short u16;
__device__ __forceinline__ float bf2f(u16 v) { return __uint_as_float(((unsigned)v) << 16); }
__device__ __forceinline__ u16 f2bf(float f) { __hip_bfloat16 b = __float2bfloat16(f); return *reinterpret_cast<u16*>(&b); }

constexpr int B = 2, H = 32, SK = 4096, D = 128, M = 512, DC = 512, F = 1376;

__device__ void layernorm_to_lds(const u16* x, float* xn  , float* red) {
    int t = threadIdx.x;
    for (int b = 0; b < 2; ++b) {
        float v0 = bf2f(x[b * M + t]), v1 = bf2f(x[b * M + 256 + t]);
        float s = v0 + v1;
        for (int o = 32; o > 0; o >>= 1) s += __shfl_xor(s, o);
        __syncthreads();
        if ((t & 63) == 0) red[t >> 6] = s;
        __syncthreads();
        float mean = (red[0] + red[1] + red[2] + red[3]) * (1.0f / M);
        float d0 = v0 - mean, d1 = v1 - mean;
        float q = d0 * d0 + d1 * d1;
        for (int o = 32; o > 0; o >>= 1) q += __shfl_xor(q, o);
        __syncthreads();
        if ((t & 63) == 0) red[t >> 6] = q;
        __syncthreads();
        float var = (red[0] + red[1] + red[2] + red[3]) * (1.0f / M);
        float r = 1.0f / sqrtf(var + 1e-5f);
        xn[b * M + t] = d0 * r;
        xn[b * M + 256 + t] = d1 * r;
    }
    __syncthreads();
}

__global__ __launch_bounds__(256) void k1(const u16* x, const u16* wq, const u16* wuk, const u16* w1, const u16* b1, const u16* w2,
                                          float* qabs  , float* ypart  ) {
    __shared__ float xn[2 * M];
    __shared__ float red[4];
    __shared__ float part[16][2][128];
    __shared__ float q[2][128];
    layernorm_to_lds(x, xn, red);
    int t = threadIdx.x;
    if (blockIdx.x < 256) {
        int h = blockIdx.x >> 3, j = blockIdx.x & 7;
        int dc = (t & 15) * 8, rg = t >> 4;
        float acc[2][8] = {};
        for (int i = 0; i < 32; ++i) {
            int m = rg + 16 * i;
            const u16* p = wq + ((size_t)m * H + h) * D + dc;
            uint4 v = *reinterpret_cast<const uint4*>(p);
            unsigned w[4] = {v.x, v.y, v.z, v.w};
            float x0 = xn[m], x1 = xn[M + m];
#pragma unroll
            for (int e = 0; e < 4; ++e) {
                float lo = __uint_as_float(w[e] << 16), hi = __uint_as_float(w[e] & 0xffff0000u);
                acc[0][2 * e] += x0 * lo; acc[0][2 * e + 1] += x0 * hi;
                acc[1][2 * e] += x1 * lo; acc[1][2 * e + 1] += x1 * hi;
            }
        }
        for (int e = 0; e < 8; ++e) { part[rg][0][dc + e] = acc[0][e]; part[rg][1][dc + e] = acc[1][e]; }
        __syncthreads();
        { int b = t >> 7, d = t & 127; float s = 0; for (int r = 0; r < 16; ++r) s += part[r][b][d]; q[b][d] = s; }
        __syncthreads();
        int c = 64 * j + (t >> 2), dq = (t & 3) * 32;
        float a0 = 0, a1 = 0;
        const u16* p = wuk + ((size_t)c * H + h) * D + dq;
        for (int e = 0; e < 32; ++e) { float w = bf2f(p[e]); a0 += w * q[0][dq + e]; a1 += w * q[1][dq + e]; }
        a0 += __shfl_xor(a0, 1); a0 += __shfl_xor(a0, 2);
        a1 += __shfl_xor(a1, 1); a1 += __shfl_xor(a1, 2);
        const float sc = 0.08838834764831845f;
        if ((t & 3) == 0) { qabs[((size_t)0 * H + h) * DC + c] = a0 * sc; qabs[((size_t)1 * H + h) * DC + c] = a1 * sc; }
    } else {
        int fs = blockIdx.x - 256;
        int f = fs * 32 + (t & 31), rg = t >> 5;
        float a0 = 0, a1 = 0;
        for (int m = rg; m < M; m += 8) { float w = bf2f(w1[(size_t)m * F + f]); a0 += w * xn[m]; a1 += w * xn[M + m]; }
        part[rg][0][t & 31] = a0; part[rg][1][t & 31] = a1;
        __syncthreads();
        if (t < 64) {
            int b = t >> 5, ff = t & 31; float s = 0; for (int r = 0; r < 8; ++r) s += part[r][b][ff];
            s += bf2f(b1[fs * 32 + ff]);
            float u = 0.7978845608028654f * (s + 0.044715f * s * s * s);
            q[b][ff] = 0.5f * s * (1.0f + tanhf(u));
        }
        __syncthreads();
        for (int m = t; m < M; m += 256) {
            float y0 = 0, y1 = 0;
            for (int ff = 0; ff < 32; ++ff) { float w = bf2f(w2[(size_t)(fs * 32 + ff) * M + m]); y0 += w * q[0][ff]; y1 += w * q[1][ff]; }
            ypart[((size_t)fs * 2 + 0) * M + m] = y0; ypart[((size_t)fs * 2 + 1) * M + m] = y1;
        }
    }
}

__global__ __launch_bounds__(256) void k2(const u16* ckv, const float* qabs, float* opart  , float* ml  ) {
    __shared__ float kv[32][DC + 1];
    __shared__ float s[32][33];
    int b = blockIdx.x >> 7, sp = blockIdx.x & 127, t = threadIdx.x;
    for (int i = t; i < 32 * DC; i += 256) { int k = i >> 9, c = i & 511; kv[k][c] = bf2f(ckv[((size_t)b * SK + sp * 32 + k) * DC + c]); }
    __syncthreads();
    for (int i = t; i < 1024; i += 256) {
        int h = i >> 5, k = i & 31;
        const float* qp = qabs + ((size_t)b * H + h) * DC;
        float a = 0;
        for (int c = 0; c < DC; ++c) a += qp[c] * kv[k][c];
        s[h][k] = a;
    }
    __syncthreads();
    if (t < 32) {
        float m = -INFINITY; for (int k = 0; k < 32; ++k) m = fmaxf(m, s[t][k]);
        float l = 0; for (int k = 0; k < 32; ++k) { float p = __expf(s[t][k] - m); s[t][k] = p; l += p; }
        ml[(((size_t)b * 128 + sp) * 32 + t) * 2] = m; ml[(((size_t)b * 128 + sp) * 32 + t) * 2 + 1] = l;
    }
    __syncthreads();
    for (int c = t; c < DC; c += 256)
        for (int h = 0; h < 32; ++h) {
            float a = 0;
            for (int k = 0; k < 32; ++k) a += s[h][k] * kv[k][c];
            opart[(((size_t)b * 128 + sp) * 32 + h) * DC + c] = a;
        }
}

__global__ __launch_bounds__(256) void k3(const float* opart, const float* ml, const u16* wuv, const u16* wo, float* spart) {
    __shared__ float wgt[2][128];
    __shared__ float cc[2][64];
    __shared__ float ctx[2][128];
    int h = blockIdx.x >> 3, j = blockIdx.x & 7, t = threadIdx.x;
    if (t < 2) {
        float Mx = -INFINITY; for (int i = 0; i < 128; ++i) Mx = fmaxf(Mx, ml[(((size_t)t * 128 + i) * 32 + h) * 2]);
        float L = 0; for (int i = 0; i < 128; ++i) { float w = __expf(ml[(((size_t)t * 128 + i) * 32 + h) * 2] - Mx); wgt[t][i] = w; L += w * ml[(((size_t)t * 128 + i) * 32 + h) * 2 + 1]; }
        for (int i = 0; i < 128; ++i) wgt[t][i] /= L;
    }
    __syncthreads();
    if (t < 128) {
        int b = t >> 6, c = t & 63; float a = 0;
        for (int i = 0; i < 128; ++i) a += wgt[b][i] * opart[(((size_t)b * 128 + i) * 32 + h) * DC + 64 * j + c];
        cc[b][c] = a;
    }
    __syncthreads();
    { int b = t >> 7, d = t & 127; float a = 0;
      for (int c = 0; c < 64; ++c) a += cc[b][c] * bf2f(wuv[((size_t)(64 * j + c) * H + h) * D + d]);
      ctx[b][d] = a; }
    __syncthreads();
    for (int m = t; m < M; m += 256) {
        float a0 = 0, a1 = 0;
        for (int d = 0; d < D; ++d) { float w = bf2f(wo[((size_t)h * D + d) * M + m]); a0 += w * ctx[0][d]; a1 += w * ctx[1][d]; }
        spart[((size_t)blockIdx.x * 2 + 0) * M + m] = a0; spart[((size_t)blockIdx.x * 2 + 1) * M + m] = a1;
    }
}

__global__ __launch_bounds__(256) void k4(const u16* x, const float* spart, const float* ypart, const u16* b2, u16* out) {
    int i = blockIdx.x * 256 + threadIdx.x;
    int m = i & 511;
    float s = 0; for (int p = 0; p < 256; ++p) s += spart[(size_t)p * 1024 + i];
    float y = bf2f(b2[m]); for (int p = 0; p < 43; ++p) y += ypart[(size_t)p * 1024 + i];
    float g = y / (1.0f + __expf(-y));
    out[i] = f2bf(bf2f(x[i]) + s * g);
}

extern "C" void kernel_launch(void* const* d_in, const int* in_sizes, int n_in, void* d_out, int out_size, void* d_ws, size_t ws_size,
                              hipStream_t stream) {
    const u16* x = (const u16*)d_in[0];
    const u16* ckv = (const u16*)d_in[1];
    const u16* wq = (const u16*)d_in[2];
    const u16* wuk = (const u16*)d_in[3];
    const u16* wuv = (const u16*)d_in[4];
    const u16* wo = (const u16*)d_in[5];
    const u16* w1 = (const u16*)d_in[7];
    const u16* b1 = (const u16*)d_in[8];
    const u16* w2 = (const u16*)d_in[9];
    const u16* b2 = (const u16*)d_in[10];
    float* ws = (float*)d_ws;
    float* qabs = ws;
    float* ypart = qabs + 32768;
    float* ml = ypart + 65536;
    float* spart = ml + 16384;
    float* opart = spart + 262144;
    k1<<<256 + 43, 256, 0, stream>>>(x, wq, wuk, w1, b1, w2, qabs, ypart);
    k2<<<256, 256, 0, stream>>>(ckv, qabs, opart, ml);
    k3<<<256, 256, 0, stream>>>(opart, ml, wuv, wo, spart);
    k4<<<4, 256, 0, stream>>>(x, spart, ypart, b2, (u16*)d_out);
}
```

```cpp
#include <hip/hip_runtime.h>
#include <stdint.h>

typedef unsigned short u16;
typedef unsigned v4u __attribute__((ext_vector_type(4)));
typedef unsigned v2u __attribute__((ext_vector_type(2)));
typedef float f32x4 __attribute__((ext_vector_type(4)));
typedef float f32x2 __attribute__((ext_vector_type(2)));
typedef __bf16 bf16x8 __attribute__((ext_vector_type(8)));
typedef __bf16 bf16x2 __attribute__((ext_vector_type(2)));
typedef short s16x4 __attribute__((ext_vector_type(4)));
typedef unsigned short u16x2 __attribute__((ext_vector_type(2)));
#define LAS __attribute__((address_space(3)))
#define GAS __attribute__((address_space(1)))
typedef __amdgpu_buffer_rsrc_t rsrc_t;

constexpr int AUX_NT = 2, AUX_SC1 = 16;
constexpr int NFFN = 86;
constexpr int WS_EPOCH = 0;
constexpr int WS_QG = 4096;
constexpr int WS_BLK = WS_QG + 4 * 32768;
constexpr int BLK_BYTES = 24576, BLK_LSE = 16384, BLK_SLOT = 16896, SLOT_BYTES = 3840;
constexpr int WS_BYTES = WS_BLK + 128 * BLK_BYTES;
constexpr unsigned SPIN_CAP = 400000u;
constexpr int LDS_BYTES = 163840;
#define CODE_LINES 130
#define A_DELAY 0

#define STAMP(k) do { } while (0)

__device__ __forceinline__ f32x4 mfma(v4u a, v4u b, f32x4 c) {
    return __builtin_amdgcn_mfma_f32_16x16x32_bf16(__builtin_bit_cast(bf16x8, a), __builtin_bit_cast(bf16x8, b), c, 0, 0, 0);
}
__device__ __forceinline__ unsigned pk_bf16(float lo, float hi) {
    f32x2 p = {lo, hi};
    return __builtin_bit_cast(unsigned, __builtin_convertvector(p, bf16x2));
}
__device__ __forceinline__ float bf_lo(unsigned u) { return __builtin_bit_cast(float, u << 16); }
__device__ __forceinline__ float bf_hi(unsigned u) { return __builtin_bit_cast(float, u & 0xffff0000u); }
__device__ __forceinline__ float rbf(float x) { return bf_lo(pk_bf16(x, x)); }
__device__ __forceinline__ unsigned nosent(unsigned u) { return u < 0xfffffffeu ? u : 0xfffffffeu; }
__device__ __forceinline__ unsigned fbits(float f) { return __builtin_bit_cast(unsigned, f); }
__device__ __forceinline__ float bitsf(unsigned u) { return __builtin_bit_cast(float, u); }

__device__ __forceinline__ unsigned pk_sub_sat(unsigned a, unsigned b) {
    return __builtin_bit_cast(unsigned, __builtin_elementwise_sub_sat(__builtin_bit_cast(u16x2, a), __builtin_bit_cast(u16x2, b)));
}
template <int CTRL> __device__ __forceinline__ float dpp(float v) {
    return __builtin_bit_cast(float, __builtin_amdgcn_update_dpp(0, __builtin_bit_cast(int, v), CTRL, 0xf, 0xf, true));
}
constexpr int DPP_X1 = 0xB1, DPP_X2 = 0x4E, DPP_HMIR = 0x141, DPP_MIR = 0x140;
__device__ __forceinline__ float sum_x16(float v) { auto r = __builtin_amdgcn_permlane16_swap(fbits(v), fbits(v), false, false); return bitsf(r[0]) + bitsf(r[1]); }
__device__ __forceinline__ float sum_x32(float v) { auto r = __builtin_amdgcn_permlane32_swap(fbits(v), fbits(v), false, false); return bitsf(r[0]) + bitsf(r[1]); }
__device__ __forceinline__ float max_x16(float v) { auto r = __builtin_amdgcn_permlane16_swap(fbits(v), fbits(v), false, false); return fmaxf(bitsf(r[0]), bitsf(r[1])); }
__device__ __forceinline__ float max_x32(float v) { auto r = __builtin_amdgcn_permlane32_swap(fbits(v), fbits(v), false, false); return fmaxf(bitsf(r[0]), bitsf(r[1])); }
__device__ __forceinline__ float sum8(float v) { v += dpp<DPP_X1>(v); v += dpp<DPP_X2>(v); v += dpp<DPP_HMIR>(v); return v; }
__device__ __forceinline__ float max8(float v) { v = fmaxf(v, dpp<DPP_X1>(v)); v = fmaxf(v, dpp<DPP_X2>(v)); v = fmaxf(v, dpp<DPP_HMIR>(v)); return v; }
__device__ __forceinline__ float sum64(float v) { v = sum8(v); v += dpp<DPP_MIR>(v); v = sum_x16(v); return sum_x32(v); }

__device__ __forceinline__ rsrc_t mkrs(const void* p, int bytes) { return __builtin_amdgcn_make_buffer_rsrc((void*)p, 0, bytes, 0x00020000); }
__device__ __forceinline__ v4u ld128(rsrc_t r, int voff, int soff, int aux_is_sc1) {
    return aux_is_sc1 ? __builtin_amdgcn_raw_buffer_load_b128(r, voff, soff, AUX_SC1) : __builtin_amdgcn_raw_buffer_load_b128(r, voff, soff, AUX_NT);
}
__device__ __forceinline__ void dma16(const void* sbase, unsigned voff, unsigned ldsdst) {
    asm volatile("s_mov_b32 m0, %2\n\ts_nop 0\n\tglobal_load_lds_dwordx4 %0, %1 nt" ::"v"(voff), "s"(sbase), "s"(ldsdst) : "memory");
}
#define VMCNT(n) asm volatile("s_waitcnt vmcnt(" #n ")" ::: "memory")
#define BARRIER() do { asm volatile("s_waitcnt lgkmcnt(0)" ::: "memory"); __builtin_amdgcn_s_barrier(); asm volatile("" ::: "memory"); } while (0)

__device__ __forceinline__ v4u tr_frag(LAS unsigned char* L, int off0, int off1) {
    s16x4 a = __builtin_amdgcn_ds_read_tr16_b64_v4i16((LAS s16x4*)(L + off0));
    s16x4 b = __builtin_amdgcn_ds_read_tr16_b64_v4i16((LAS s16x4*)(L + off1));
    v2u ua = __builtin_bit_cast(v2u, a), ub = __builtin_bit_cast(v2u, b);
    return v4u{ua.x, ua.y, ub.x, ub.y};
}

__device__ __forceinline__ void layernorm16(v4u xr0, v4u xr1, const v2u (&xq)[4], int row, unsigned (&xp)[8]) {
    float a[8], b[8];
#pragma unroll
    for (int e = 0; e < 4; ++e) { a[2 * e] = bf_lo(xr0[e]); a[2 * e + 1] = bf_hi(xr0[e]); b[2 * e] = bf_lo(xr1[e]); b[2 * e + 1] = bf_hi(xr1[e]); }
    float s0 = ((a[0] + a[1]) + (a[2] + a[3])) + ((a[4] + a[5]) + (a[6] + a[7]));
    float s1 = ((b[0] + b[1]) + (b[2] + b[3])) + ((b[4] + b[5]) + (b[6] + b[7]));
    const float m0 = sum64(s0) * (1.0f / 512.0f), m1 = sum64(s1) * (1.0f / 512.0f);
    float q0 = 0.f, q1 = 0.f;
#pragma unroll
    for (int e = 0; e < 8; ++e) { const float d0 = a[e] - m0, d1 = b[e] - m1; q0 = fmaf(d0, d0, q0); q1 = fmaf(d1, d1, q1); }
    const float v0 = sum64(q0) * (1.0f / 512.0f), v1 = sum64(q1) * (1.0f / 512.0f);
    const float mean = rbf(row ? m1 : m0);
    const float sd = rbf(__builtin_sqrtf(rbf(rbf(row ? v1 : v0) + 1e-5f)));
    const float rc = __builtin_amdgcn_rcpf(sd);
#pragma unroll
    for (int k = 0; k < 4; ++k)
#pragma unroll
        for (int e = 0; e < 2; ++e) {
            const unsigned d = pk_bf16(bf_lo(xq[k][e]) - mean, bf_hi(xq[k][e]) - mean);
            const float n0 = bf_lo(d), n1 = bf_hi(d);
            float t0 = n0 * rc, t1 = n1 * rc;
            t0 = fmaf(fmaf(-t0, sd, n0), rc, t0);
            t1 = fmaf(fmaf(-t1, sd, n1), rc, t1);
            xp[2 * k + e] = pk_bf16(t0, t1);
        }
}

__global__ void __launch_bounds__(512, 2)
fused_mla_ffn(const u16* __restrict__ x, const u16* __restrict__ ckv, const u16* __restrict__ wq, const u16* __restrict__ wuk,
              const u16* __restrict__ wuv, const u16* __restrict__ wo, const u16* __restrict__ w1, const u16* __restrict__ b1,
              const u16* __restrict__ w2, const u16* __restrict__ b2, u16* __restrict__ out, unsigned char* ws) {
    __shared__ __attribute__((aligned(16))) unsigned char smem[LDS_BYTES];
    LAS unsigned char* L = (LAS unsigned char*)smem;
    STAMP(0);
    const int tid = threadIdx.x, lane = tid & 63, w = __builtin_amdgcn_readfirstlane(tid >> 6), i = lane & 15, g = lane >> 4;
    const int bid = blockIdx.x, xcd = bid & 7, idx = bid >> 3;
    const rsrc_t rsW = mkrs(ws, WS_BYTES);
    const rsrc_t rsX = mkrs(x, 2048);

    unsigned codepf = 0;
    {
        unsigned long long pc;
        asm volatile("s_getpc_b64 %0" : "=s"(pc));
        const rsrc_t rsC = mkrs((const void*)(pc & ~127ull), CODE_LINES * 128);
        codepf = __builtin_amdgcn_raw_buffer_load_b32(rsC, tid * 128, 0, 0);
    }
    const v4u xr0 = __builtin_amdgcn_raw_buffer_load_b128(rsX, lane * 16, 0, 0);
    const v4u xr1 = __builtin_amdgcn_raw_buffer_load_b128(rsX, lane * 16 + 1024, 0, 0);

    if (idx < 16) {
        const int h = 4 * xcd + (idx >> 2), cq = idx & 3, fb = h * 4 + cq;
        const int blk = WS_BLK + fb * BLK_BYTES;
        v2u xq[4];
#pragma unroll
        for (int k = 0; k < 4; ++k)
            xq[k] = __builtin_amdgcn_raw_buffer_load_b64(rsX, (i >> 3) * 1024 + (64 * w + 16 * g) * 2, ((k + cq) & 3) * 8, 0);
        const rsrc_t rsQ = mkrs(wq, 512 * 8192);
        const int qoff = (64 * w + 16 * g) * 8192 + h * 256 + i * 16;
        v4u a[16];
#pragma unroll
        for (int u = 0; u < 8; ++u) a[u] = ld128(rsQ, qoff, ((u + 4 * cq) & 15) * 8192, 0);
        __builtin_amdgcn_sched_barrier(0);
        unsigned xp[8];
        layernorm16(xr0, xr1, xq, i >> 3, xp);
        asm volatile("" ::"v"(codepf));
        __builtin_amdgcn_sched_barrier(0);
        STAMP(1);
#pragma unroll
        for (int k = 0; k < 3; ++k)
            __builtin_amdgcn_raw_buffer_store_b128(v4u{~0u, ~0u, ~0u, ~0u}, rsW, tid * 16 + k * 8192, blk, AUX_SC1);
#pragma unroll
        for (int u = 8; u < 16; ++u) a[u] = ld128(rsQ, qoff, ((u + 4 * cq) & 15) * 8192, 0);
        const rsrc_t rsK = mkrs(wuk, 512 * 8192);
        v4u uk[4];
#pragma unroll
        for (int s = 0; s < 4; ++s) uk[s] = ld128(rsK, (16 * w + i) * 8192 + g * 16 + s * 64, cq * 128 * 8192 + h * 256, 0);
        const unsigned tag = __builtin_amdgcn_raw_buffer_load_b32(rsW, WS_EPOCH, 0, AUX_SC1) + 1u;
        __builtin_amdgcn_sched_barrier(0);
        unsigned sel[2][4];
#pragma unroll
        for (int k = 0; k < 4; ++k) {
            const bool mine = ((i & 7) >> 1) == k;
            sel[0][k] = mine ? ((i & 1) ? 0x01000c0cu : 0x0c0c0100u) : 0x0c0c0c0cu;
            sel[1][k] = mine ? ((i & 1) ? 0x03020c0cu : 0x0c0c0302u) : 0x0c0c0c0cu;
        }
        f32x4 acc = {0.f, 0.f, 0.f, 0.f};
#pragma unroll
        for (int u = 0; u < 16; ++u) {
            v4u af;
#pragma unroll
            for (int k = 0; k < 4; ++k) af[k] = __builtin_amdgcn_perm(xp[u >> 1], xp[u >> 1], sel[u & 1][k]);
            acc = mfma(af, a[u], acc);
        }
        constexpr int S_PART = 131072, S_QB = S_PART + 8192;
        *(LAS f32x4*)(L + S_PART + (w * 256 + (g >> 1) * 128 + 8 * i + 4 * (g & 1)) * 4) = acc;
        VMCNT(0);
        STAMP(2);
        BARRIER();
        if (tid < 128) {
            f32x2 s = {0.f, 0.f};
#pragma unroll
            for (int ww = 0; ww < 8; ++ww) s += *(LAS f32x2*)(L + S_PART + ww * 1024 + tid * 8);
            *(LAS unsigned*)(L + S_QB + tid * 4) = pk_bf16(s.x, s.y);
        }
        BARRIER();
        {
            f32x4 d = {0.f, 0.f, 0.f, 0.f};
#pragma unroll
            for (int s = 0; s < 4; ++s) d = mfma(uk[s], *(LAS v4u*)(L + S_QB + (i & 1) * 256 + s * 64 + g * 16), d);
            if (i < 2) {
                const int P = ((2 * w + (g >> 1)) * 64 + 16 * cq + (h & 15)) * 2 + (g & 1);
                __builtin_amdgcn_raw_buffer_store_b128(v4u{pk_bf16(d.x, d.y), tag, pk_bf16(d.z, d.w), tag}, rsW,
                                                       WS_QG + ((i * 2 + (h >> 4)) * 2048 + P) * 16, 0, AUX_SC1);
            }
        }
        STAMP(3);
        BARRIER();
        constexpr int S_UV = 131072;
        {
            const unsigned char* base = (const unsigned char*)wuv + (size_t)(cq * 128 + 16 * w) * 8192 + h * 256;
#pragma unroll
            for (int j = 0; j < 4; ++j) {
                const int row = 4 * j + g;
                dma16(base, row * 8192 + ((i ^ ((row & 7) << 1)) * 16), S_UV + (16 * w + 4 * j) * 256);
            }
            const unsigned char* bo = (const unsigned char*)wo + (size_t)h * 131072 + w * 128;
            const int r8 = lane >> 3;
            const unsigned vo = r8 * 1024 + (((lane & 7) ^ (((r8 >> 1) & 3) << 1)) * 16);
#pragma unroll
            for (int j = 0; j < 16; ++j) {
                const int rg = (j + 4 * cq) & 15;
                dma16(bo + rg * 8192, vo, w * 16384 + rg * 1024);
            }
        }
        const int q4 = (lane >> 2) & 3, p4 = lane & 3, trow = 4 * g + q4;
        v4u fuv[4], fo[4][4];
        VMCNT(16);
        BARRIER();
#pragma unroll
        for (int s = 0; s < 4; ++s) {
            const int o = S_UV + (32 * s + trow) * 256 + (((2 * w + (p4 >> 1)) ^ ((trow & 7) << 1)) * 16) + 8 * (p4 & 1);
            fuv[s] = tr_frag(L, o, o + 16 * 256);
        }
#pragma unroll
        for (int s = 0; s < 4; ++s) {
            if (s == 0) VMCNT(12); else if (s == 1) VMCNT(8); else if (s == 2) VMCNT(4); else VMCNT(0);
            const int sb = w * 16384 + (((s + cq) & 3) * 32 + trow) * 128 + 8 * (p4 & 1);
#pragma unroll
            for (int T = 0; T < 4; ++T) {
                const int o = sb + (((2 * T + (p4 >> 1)) ^ (((trow >> 1) & 3) << 1)) * 16);
                fo[s][T] = tr_frag(L, o, o + 16 * 128);
            }
        }
        BARRIER();
        STAMP(4);
        v4u pr0, pr1;
        unsigned la, lo;
        for (unsigned it = 0;; ++it) {
            pr0 = ld128(rsW, w * 1024 + lane * 16, blk, 1);
            pr1 = ld128(rsW, w * 1024 + lane * 16 + 8192, blk, 1);
            la = __builtin_amdgcn_raw_buffer_load_b32(rsW, BLK_LSE + lane * 4, blk, AUX_SC1);
            lo = __builtin_amdgcn_raw_buffer_load_b32(rsW, BLK_LSE + (16 * g + (i & 8) + w) * 4, blk, AUX_SC1);
            unsigned mx = max(max(max(pr0.x, pr0.y), max(pr0.z, pr0.w)), max(max(pr1.x, pr1.y), max(pr1.z, pr1.w)));
            mx = max(mx, max(la, lo));
            if (__all(mx != ~0u) || it > SPIN_CAP) break;
            asm volatile("" ::: "memory");
        }
        STAMP(5);
        const float Mx = max_x32(max_x16(max8(bitsf(la))));
        const float wgt = __builtin_amdgcn_exp2f(bitsf(lo) - Mx);
        constexpr int S_CC = 131072, CC_ROW = 272, S_CTX = S_CC + 16 * CC_ROW;
        {
            const unsigned wp = pk_bf16(wgt, wgt);
            v4u af;
#pragma unroll
            for (int k = 0; k < 4; ++k) af[k] = __builtin_amdgcn_perm(wp, wp, sel[0][k]);
            const v4u z = {0u, 0u, 0u, 0u};
            f32x4 d = {0.f, 0.f, 0.f, 0.f};
            d = mfma(i < 8 ? af : z, pr0, d);
            d = mfma(i < 8 ? z : af, pr1, d);
            *(LAS v2u*)(L + S_CC + (8 * (g >> 1) + w) * CC_ROW + (8 * i + 4 * (g & 1)) * 2) = v2u{pk_bf16(d.x, d.y), pk_bf16(d.z, d.w)};
        }
        const float Lsum = sum_x32(sum_x16(sum8(__builtin_amdgcn_exp2f(bitsf(la) - Mx))));
        const float rL = 1.0f / Lsum;
        BARRIER();
        {
            f32x4 d = {0.f, 0.f, 0.f, 0.f};
#pragma unroll
            for (int s = 0; s < 4; ++s) d = mfma(fuv[s], *(LAS v4u*)(L + S_CC + i * CC_ROW + s * 64 + g * 16), d);
            d.x = sum8(d.x) * rL; d.y = sum8(d.y) * rL; d.z = sum8(d.z) * rL; d.w = sum8(d.w) * rL;
            if ((i & 7) == 0)
                *(LAS v2u*)(L + S_CTX + (i >> 3) * 256 + (32 * (w >> 1) + 8 * g + 4 * (w & 1)) * 2) = v2u{pk_bf16(d.x, d.y), pk_bf16(d.z, d.w)};
        }
        BARRIER();
        {
            v4u cf[4];
#pragma unroll
            for (int s = 0; s < 4; ++s) cf[s] = *(LAS v4u*)(L + S_CTX + (i & 1) * 256 + ((s + cq) & 3) * 64 + g * 16);
#pragma unroll
            for (int T = 0; T < 4; ++T) {
                f32x4 d = {0.f, 0.f, 0.f, 0.f};
#pragma unroll
                for (int s = 0; s < 4; ++s) d = mfma(fo[s][T], cf[s], d);
                if (i < 2) {
                    const int slot = i * 128 + 16 * w + 4 * T + g;
                    __builtin_amdgcn_raw_buffer_store_b128(v4u{nosent(fbits(d.x)), nosent(fbits(d.y)), nosent(fbits(d.z)), nosent(fbits(d.w))}, rsW,
                                                           WS_BLK + (slot >> 1) * BLK_BYTES + BLK_SLOT + (slot & 1) * SLOT_BYTES, fb * 16, AUX_SC1);
                }
            }
        }
        STAMP(6);
    } else {
        const int hh = idx & 1, spa = xcd * 8 + ((idx - 16) >> 1), b = spa >> 5, sp = spa & 31, aidx = spa * 2 + hh;
        v2u xq[4];
#pragma unroll
        for (int k = 0; k < 4; ++k) xq[k] = __builtin_amdgcn_raw_buffer_load_b64(rsX, (i & 1) * 1024 + (64 * w + 4 * g) * 2 + k * 32, 0, 0);
        const unsigned tag = __builtin_amdgcn_raw_buffer_load_b32(rsW, WS_EPOCH, 0, AUX_SC1) + 1u;
        unsigned xp[8];
        layernorm16(xr0, xr1, xq, i & 1, xp);
        asm volatile("" ::"v"(codepf), "v"(tag));
        __builtin_amdgcn_sched_barrier(0);
        if (A_DELAY) __builtin_amdgcn_s_sleep(A_DELAY);
        STAMP(1);
        {
            const unsigned char* base = (const unsigned char*)ckv + ((size_t)spa * 128 + 16 * w) * 1024;
#pragma unroll
            for (int j = 0; j < 16; ++j) {
                const int r = (j + 8 * hh) & 15;
                const int X = ((r & 7) << 1) | (r >> 3);
                dma16(base + r * 1024, (lane ^ X) * 16, (16 * w + r) * 1024);
            }
        }
        const int q4 = (lane >> 2) & 3, p4 = lane & 3, trow = 4 * g + q4;
        v4u kf[16], vf[4][4];
        VMCNT(0);
        STAMP(2);
        {
            const int X = ((i & 7) << 1) | (i >> 3);
#pragma unroll
            for (int s = 0; s < 16; ++s) kf[s] = *(LAS v4u*)(L + (16 * w + i) * 1024 + g * 256 + ((s ^ X) * 16));
        }
        BARRIER();
        {
            const int X = ((trow & 7) << 1) | (trow >> 3);
#pragma unroll
            for (int T = 0; T < 4; ++T) {
                const int o = trow * 1024 + (w >> 1) * 256 + (((8 * (w & 1) + 2 * T + (p4 >> 1)) ^ X) * 16) + 8 * (p4 & 1);
#pragma unroll
                for (int s = 0; s < 4; ++s) vf[s][T] = tr_frag(L, o + s * 32768, o + s * 32768 + 16384);
            }
        }
        asm volatile("s_waitcnt lgkmcnt(0)" ::: "memory");
        STAMP(3);
        constexpr int S_Q = 131072, S_P = S_Q + 16384, S_M = S_P + 4096, S_L = S_M + 512;
        {
            v4u qg[4];
            const int qb = WS_QG + (b * 2 + hh) * 32768 + w * 4096;
            const int tb = WS_QG + (b * 2 + (hh ^ 1)) * 32768 + w * 256;
            for (unsigned it = 0;; ++it) {
#pragma unroll
                for (int t = 0; t < 4; ++t) qg[t] = ld128(rsW, lane * 16 + t * 1024, qb, 1);
                const v2u tk = __builtin_amdgcn_raw_buffer_load_b64(rsW, (lane & 7) * 32, tb, AUX_SC1);
                unsigned bad = tk.y ^ tag;
#pragma unroll
                for (int t = 0; t < 4; ++t) bad |= (qg[t].y ^ tag) | (qg[t].w ^ tag);
                if (__all(bad == 0u) || it > SPIN_CAP) break;
                asm volatile("" ::: "memory");
            }
#pragma unroll
            for (int t = 0; t < 4; ++t) *(LAS v2u*)(L + S_Q + w * 2048 + t * 512 + lane * 8) = v2u{qg[t].x, qg[t].z};
        }
        STAMP(4);
        BARRIER();
        const bool ffn = aidx < NFFN;
        v4u w2r[4];
        unsigned b1v = 0;
        if (ffn) {
            const unsigned char* base = (const unsigned char*)w1 + (size_t)(64 * w) * 2752 + aidx * 32;
            dma16(base, (lane >> 1) * 2752 + (lane & 1) * 16, w * 2048);
            dma16(base + 32 * 2752, (lane >> 1) * 2752 + (lane & 1) * 16, w * 2048 + 1024);
        }
        {
            f32x4 d = {0.f, 0.f, 0.f, 0.f};
            v4u qf[16];
#pragma unroll
            for (int s = 0; s < 16; ++s) qf[s] = *(LAS v4u*)(L + S_Q + s * 1024 + lane * 16);
            __builtin_amdgcn_sched_barrier(0);
#pragma unroll
            for (int s = 0; s < 16; ++s) d = mfma(kf[s], qf[s], d);
            const float SC = 0.08838834764831845f, LOG2E = 1.4426950408889634f;
            const unsigned r0 = pk_bf16(d.x, d.y), r1 = pk_bf16(d.z, d.w);
            const unsigned s0 = pk_bf16(bf_lo(r0) * SC, bf_hi(r0) * SC), s1 = pk_bf16(bf_lo(r1) * SC, bf_hi(r1) * SC);
            const float t0 = bf_lo(s0), t1 = bf_hi(s0), t2 = bf_lo(s1), t3 = bf_hi(s1);
            const float mt = max_x32(max_x16(fmaxf(fmaxf(t0, t1), fmaxf(t2, t3))));
            const float me = __builtin_ceilf(mt * LOG2E);
            const float p0 = __builtin_amdgcn_exp2f(fmaf(t0, LOG2E, -me)), p1 = __builtin_amdgcn_exp2f(fmaf(t1, LOG2E, -me));
            const float p2 = __builtin_amdgcn_exp2f(fmaf(t2, LOG2E, -me)), p3 = __builtin_amdgcn_exp2f(fmaf(t3, LOG2E, -me));
            *(LAS v2u*)(L + S_P + (w >> 1) * 1024 + lane * 16 + (w & 1) * 8) = v2u{pk_bf16(p0, p1), pk_bf16(p2, p3)};
            const float ls = sum_x32(sum_x16((p0 + p1) + (p2 + p3)));
            if (g == 0) { *(LAS float*)(L + S_M + i * 32 + w * 4) = me; *(LAS float*)(L + S_L + i * 32 + w * 4) = ls; }
        }
        BARRIER();
        STAMP(5);
        {
            const f32x4 ma = *(LAS f32x4*)(L + S_M + i * 32), mb = *(LAS f32x4*)(L + S_M + i * 32 + 16);
            v4u pf[4];
#pragma unroll
            for (int s = 0; s < 4; ++s) pf[s] = *(LAS v4u*)(L + S_P + s * 1024 + lane * 16);
            const f32x4 la = *(LAS f32x4*)(L + S_L + i * 32), lb = *(LAS f32x4*)(L + S_L + i * 32 + 16);
            const float Mx = fmaxf(fmaxf(fmaxf(ma.x, ma.y), fmaxf(ma.z, ma.w)), fmaxf(fmaxf(mb.x, mb.y), fmaxf(mb.z, mb.w)));
            const float me[8] = {ma.x, ma.y, ma.z, ma.w, mb.x, mb.y, mb.z, mb.w};
            const float ls[8] = {la.x, la.y, la.z, la.w, lb.x, lb.y, lb.z, lb.w};
            float lsum = 0.f;
#pragma unroll
            for (int t = 0; t < 8; ++t) {
                const float df = fminf(Mx - me[t], 255.0f);
                const unsigned e = (unsigned)df * 0x00800080u;
                pf[t >> 1][2 * (t & 1)] = pk_sub_sat(pf[t >> 1][2 * (t & 1)], e);
                pf[t >> 1][2 * (t & 1) + 1] = pk_sub_sat(pf[t >> 1][2 * (t & 1) + 1], e);
                lsum = fmaf(ls[t], __builtin_amdgcn_exp2f(-df), lsum);
            }
            const float rl = 1.0f / lsum;
            const int rec = WS_BLK + ((16 * hh + i) * 4 + (w >> 1)) * BLK_BYTES + (8 * (w & 1) + g) * 16;
#pragma unroll
            for (int tp = 0; tp < 2; ++tp) {
                f32x4 d0 = {0.f, 0.f, 0.f, 0.f}, d1 = {0.f, 0.f, 0.f, 0.f};
#pragma unroll
                for (int s = 0; s < 4; ++s) { d0 = mfma(vf[s][2 * tp], pf[s], d0); d1 = mfma(vf[s][2 * tp + 1], pf[s], d1); }
                d0 *= rl; d1 *= rl;
                __builtin_amdgcn_raw_buffer_store_b128(v4u{nosent(pk_bf16(d0.x, d0.y)), nosent(pk_bf16(d0.z, d0.w)), nosent(pk_bf16(d1.x, d1.y)), nosent(pk_bf16(d1.z, d1.w))},
                                                       rsW, rec + tp * 64, spa * 256, AUX_SC1);
            }
            if (w == 0)
                __builtin_amdgcn_raw_buffer_store_b32(nosent(fbits(Mx + __builtin_amdgcn_logf(lsum))), rsW, WS_BLK + ((16 * hh + i) * 4 + g) * BLK_BYTES + BLK_LSE,
                                                      (16 * (sp & 3) + 8 * b + (sp >> 2)) * 4, AUX_SC1);
        }
        STAMP(6);
        if (ffn) {
            constexpr int S_HP = 16384, S_H = S_HP + 1024;
            if (w < 4) {
                const rsrc_t rs2 = mkrs(w2, 1376 * 1024);
#pragma unroll
                for (int u = 0; u < 4; ++u) w2r[u] = ld128(rs2, (4 * g + u) * 1024 + i * 16, aidx * 16384 + w * 256, 0);
            }
            if (tid < 32) b1v = b1[aidx * 16 + (tid & 15)];
            VMCNT(0);
            f32x4 d = {0.f, 0.f, 0.f, 0.f};
#pragma unroll
            for (int s = 0; s < 2; ++s) {
                const int o = w * 2048 + s * 1024 + trow * 32 + p4 * 8;
                d = mfma(tr_frag(L, o, o + 512), v4u{xp[4 * s], xp[4 * s + 1], xp[4 * s + 2], xp[4 * s + 3]}, d);
            }
            if (i < 2) *(LAS f32x4*)(L + S_HP + w * 128 + i * 64 + g * 16) = d;
            BARRIER();
            if (tid < 32) {
                float s = 0.f;
#pragma unroll
                for (int ww = 0; ww < 8; ++ww) s += *(LAS float*)(L + S_HP + ww * 128 + tid * 4);
                const float hv = rbf(rbf(s) + bf_lo(b1v));
                const float u2 = 2.0f * 0.7978845608028654f * 1.4426950408889634f * (hv + 0.044715f * hv * hv * hv);
                const float th = 1.0f - 2.0f / (__builtin_amdgcn_exp2f(u2) + 1.0f);
                *(LAS u16*)(L + S_H + tid * 2) = (u16)pk_bf16(0.5f * hv * (1.0f + th), 0.f);
            }
            BARRIER();
            if (w < 4) {
                const v2u hp = *(LAS v2u*)(L + S_H + (i >> 3) * 32 + g * 8);
                f32x4 y = {0.f, 0.f, 0.f, 0.f};
#pragma unroll
                for (int u = 0; u < 4; ++u) {
                    v4u af;
#pragma unroll
                    for (int k = 0; k < 4; ++k) {
                        const bool mine = ((i & 7) >> 1) == k;
                        const unsigned sl = (u & 1) ? (mine ? ((i & 1) ? 0x03020c0cu : 0x0c0c0302u) : 0x0c0c0c0cu) : (mine ? ((i & 1) ? 0x01000c0cu : 0x0c0c0100u) : 0x0c0c0c0cu);
                        af[k] = __builtin_amdgcn_perm(hp[u >> 1], hp[u >> 1], sl);
                    }
                    y = mfma(af, w2r[u], y);
                }
                const int slot = (g >> 1) * 128 + 32 * w + 2 * i + (g & 1);
                __builtin_amdgcn_raw_buffer_store_b128(v4u{nosent(fbits(y.x)), nosent(fbits(y.y)), nosent(fbits(y.z)), nosent(fbits(y.w))}, rsW,
                                                       WS_BLK + (slot >> 1) * BLK_BYTES + BLK_SLOT + (slot & 1) * SLOT_BYTES, (128 + aidx) * 16, AUX_SC1);
            }
        }
        STAMP(7);
        if (w < 2) {
            const int slot = 2 * aidx + w;
            const int sb = WS_BLK + aidx * BLK_BYTES + BLK_SLOT + w * SLOT_BYTES;
            const v2u xo = __builtin_amdgcn_raw_buffer_load_b64(rsX, slot * 8, 0, 0);
            const v2u bo = *(const v2u*)(b2 + (slot & 127) * 4);
            v4u r0, r1;
            const int l2 = lane < NFFN - 64 ? lane : 0;
            for (unsigned it = 0;; ++it) {
                r0 = ld128(rsW, 2048 + lane * 16, sb, 1);
                r1 = ld128(rsW, 3072 + l2 * 16, sb, 1);
                const unsigned mx = max(max(max(r0.x, r0.y), max(r0.z, r0.w)), max(max(r1.x, r1.y), max(r1.z, r1.w)));
                if (__all(mx != ~0u) || it > SPIN_CAP) break;
                asm volatile("" ::: "memory");
            }
            float gt[4];
#pragma unroll
            for (int e = 0; e < 4; ++e) {
                const float ys = sum64(bitsf(r0[e]) + (lane < NFFN - 64 ? bitsf(r1[e]) : 0.f));
                const float bias = (e & 1) ? bf_hi(bo[e >> 1]) : bf_lo(bo[e >> 1]);
                const float y = rbf(rbf(ys) + bias);
                gt[e] = rbf(y / (1.0f + __builtin_amdgcn_exp2f(-1.4426950408889634f * y)));
            }
            STAMP(8);
            for (unsigned it = 0;; ++it) {
                r0 = ld128(rsW, lane * 16, sb, 1);
                r1 = ld128(rsW, 1024 + lane * 16, sb, 1);
                const unsigned mx = max(max(max(r0.x, r0.y), max(r0.z, r0.w)), max(max(r1.x, r1.y), max(r1.z, r1.w)));
                if (__all(mx != ~0u) || it > SPIN_CAP) break;
                asm volatile("" ::: "memory");
            }
            STAMP(9);
            float o[4];
#pragma unroll
            for (int e = 0; e < 4; ++e) {
                const float s = rbf(sum64(bitsf(r0[e]) + bitsf(r1[e])));
                const float xv = (e & 1) ? bf_hi(xo[e >> 1]) : bf_lo(xo[e >> 1]);
                o[e] = xv + rbf(s * gt[e]);
            }
            if (lane == 0) {
                *(v2u*)(out + slot * 4) = v2u{pk_bf16(o[0], o[1]), pk_bf16(o[2], o[3])};
                if (slot == 0) __builtin_amdgcn_raw_buffer_store_b32(tag, rsW, WS_EPOCH, 0, AUX_SC1);
            }
            STAMP(10);
        }
    }
}

extern "C" void kernel_launch(void* const* d_in, const int* in_sizes, int n_in, void* d_out, int out_size, void* d_ws, size_t ws_size,
                              hipStream_t stream) {
    fused_mla_ffn<<<256, 512, 0, stream>>>((const u16*)d_in[0], (const u16*)d_in[1], (const u16*)d_in[2], (const u16*)d_in[3], (const u16*)d_in[4],
                                           (const u16*)d_in[5], (const u16*)d_in[7], (const u16*)d_in[8], (const u16*)d_in[9], (const u16*)d_in[10],
                                           (u16*)d_out, (unsigned char*)d_ws);
}
```

```cpp
#define STAMPS
#define PROBE_K 0
#define PROBE_ROLE 0
#include <hip/hip_runtime.h>
#include <stdint.h>

typedef unsigned short u16;
typedef unsigned v4u __attribute__((ext_vector_type(4)));
typedef unsigned v2u __attribute__((ext_vector_type(2)));
typedef float f32x4 __attribute__((ext_vector_type(4)));
typedef float f32x2 __attribute__((ext_vector_type(2)));
typedef __bf16 bf16x8 __attribute__((ext_vector_type(8)));
typedef __bf16 bf16x2 __attribute__((ext_vector_type(2)));
typedef short s16x4 __attribute__((ext_vector_type(4)));
typedef unsigned short u16x2 __attribute__((ext_vector_type(2)));
#define LAS __attribute__((address_space(3)))
#define GAS __attribute__((address_space(1)))
typedef __amdgpu_buffer_rsrc_t rsrc_t;

constexpr int AUX_NT = 2, AUX_SC1 = 16;
constexpr int NFFN = 86;
constexpr int WS_EPOCH = 0;
constexpr int WS_QG = 4096;
constexpr int WS_BLK = WS_QG + 4 * 32768;
constexpr int BLK_BYTES = 24576, BLK_LSE = 16384, BLK_SLOT = 16896, SLOT_BYTES = 3840;
constexpr int WS_BYTES = WS_BLK + 128 * BLK_BYTES;
constexpr unsigned SPIN_CAP = 400000u;
constexpr int LDS_BYTES = 163840;
#define CODE_LINES 130
#define A_DELAY 0

#define NSTAMP 12
#define STAMP(k) do { __builtin_amdgcn_sched_barrier(0); stamp[k] = __builtin_amdgcn_s_memrealtime(); __builtin_amdgcn_sched_barrier(0); } while (0)

__device__ __forceinline__ f32x4 mfma(v4u a, v4u b, f32x4 c) {
    return __builtin_amdgcn_mfma_f32_16x16x32_bf16(__builtin_bit_cast(bf16x8, a), __builtin_bit_cast(bf16x8, b), c, 0, 0, 0);
}
__device__ __forceinline__ unsigned pk_bf16(float lo, float hi) {
    f32x2 p = {lo, hi};
    return __builtin_bit_cast(unsigned, __builtin_convertvector(p, bf16x2));
}
__device__ __forceinline__ float bf_lo(unsigned u) { return __builtin_bit_cast(float, u << 16); }
__device__ __forceinline__ float bf_hi(unsigned u) { return __builtin_bit_cast(float, u & 0xffff0000u); }
__device__ __forceinline__ float rbf(float x) { return bf_lo(pk_bf16(x, x)); }
__device__ __forceinline__ unsigned nosent(unsigned u) { return u < 0xfffffffeu ? u : 0xfffffffeu; }
__device__ __forceinline__ unsigned fbits(float f) { return __builtin_bit_cast(unsigned, f); }
__device__ __forceinline__ float bitsf(unsigned u) { return __builtin_bit_cast(float, u); }

__device__ __forceinline__ unsigned pk_sub_sat(unsigned a, unsigned b) {
    return __builtin_bit_cast(unsigned, __builtin_elementwise_sub_sat(__builtin_bit_cast(u16x2, a), __builtin_bit_cast(u16x2, b)));
}
template <int CTRL> __device__ __forceinline__ float dpp(float v) {
    return __builtin_bit_cast(float, __builtin_amdgcn_update_dpp(0, __builtin_bit_cast(int, v), CTRL, 0xf, 0xf, true));
}
constexpr int DPP_X1 = 0xB1, DPP_X2 = 0x4E, DPP_HMIR = 0x141, DPP_MIR = 0x140;
__device__ __forceinline__ float sum_x16(float v) { auto r = __builtin_amdgcn_permlane16_swap(fbits(v), fbits(v), false, false); return bitsf(r[0]) + bitsf(r[1]); }
__device__ __forceinline__ float sum_x32(float v) { auto r = __builtin_amdgcn_permlane32_swap(fbits(v), fbits(v), false, false); return bitsf(r[0]) + bitsf(r[1]); }
__device__ __forceinline__ float max_x16(float v) { auto r = __builtin_amdgcn_permlane16_swap(fbits(v), fbits(v), false, false); return fmaxf(bitsf(r[0]), bitsf(r[1])); }
__device__ __forceinline__ float max_x32(float v) { auto r = __builtin_amdgcn_permlane32_swap(fbits(v), fbits(v), false, false); return fmaxf(bitsf(r[0]), bitsf(r[1])); }
__device__ __forceinline__ float sum8(float v) { v += dpp<DPP_X1>(v); v += dpp<DPP_X2>(v); v += dpp<DPP_HMIR>(v); return v; }
__device__ __forceinline__ float max8(float v) { v = fmaxf(v, dpp<DPP_X1>(v)); v = fmaxf(v, dpp<DPP_X2>(v)); v = fmaxf(v, dpp<DPP_HMIR>(v)); return v; }
__device__ __forceinline__ float sum64(float v) { v = sum8(v); v += dpp<DPP_MIR>(v); v = sum_x16(v); return sum_x32(v); }

__device__ __forceinline__ rsrc_t mkrs(const void* p, int bytes) { return __builtin_amdgcn_make_buffer_rsrc((void*)p, 0, bytes, 0x00020000); }
__device__ __forceinline__ v4u ld128(rsrc_t r, int voff, int soff, int aux_is_sc1) {
    return aux_is_sc1 ? __builtin_amdgcn_raw_buffer_load_b128(r, voff, soff, AUX_SC1) : __builtin_amdgcn_raw_buffer_load_b128(r, voff, soff, AUX_NT);
}
__device__ __forceinline__ void dma16(const void* sbase, unsigned voff, unsigned ldsdst) {
    asm volatile("s_mov_b32 m0, %2\n\ts_nop 0\n\tglobal_load_lds_dwordx4 %0, %1 nt" ::"v"(voff), "s"(sbase), "s"(ldsdst) : "memory");
}
#define VMCNT(n) asm volatile("s_waitcnt vmcnt(" #n ")" ::: "memory")
#define BARRIER() do { asm volatile("s_waitcnt lgkmcnt(0)" ::: "memory"); __builtin_amdgcn_s_barrier(); asm volatile("" ::: "memory"); } while (0)

__device__ __forceinline__ v4u tr_frag(LAS unsigned char* L, int off0, int off1) {
    s16x4 a = __builtin_amdgcn_ds_read_tr16_b64_v4i16((LAS s16x4*)(L + off0));
    s16x4 b = __builtin_amdgcn_ds_read_tr16_b64_v4i16((LAS s16x4*)(L + off1));
    v2u ua = __builtin_bit_cast(v2u, a), ub = __builtin_bit_cast(v2u, b);
    return v4u{ua.x, ua.y, ub.x, ub.y};
}

__device__ __forceinline__ void layernorm16(v4u xr0, v4u xr1, const v2u (&xq)[4], int row, unsigned (&xp)[8]) {
    float a[8], b[8];
#pragma unroll
    for (int e = 0; e < 4; ++e) { a[2 * e] = bf_lo(xr0[e]); a[2 * e + 1] = bf_hi(xr0[e]); b[2 * e] = bf_lo(xr1[e]); b[2 * e + 1] = bf_hi(xr1[e]); }
    float s0 = ((a[0] + a[1]) + (a[2] + a[3])) + ((a[4] + a[5]) + (a[6] + a[7]));
    float s1 = ((b[0] + b[1]) + (b[2] + b[3])) + ((b[4] + b[5]) + (b[6] + b[7]));
    const float m0 = sum64(s0) * (1.0f / 512.0f), m1 = sum64(s1) * (1.0f / 512.0f);
    float q0 = 0.f, q1 = 0.f;
#pragma unroll
    for (int e = 0; e < 8; ++e) { const float d0 = a[e] - m0, d1 = b[e] - m1; q0 = fmaf(d0, d0, q0); q1 = fmaf(d1, d1, q1); }
    const float v0 = sum64(q0) * (1.0f / 512.0f), v1 = sum64(q1) * (1.0f / 512.0f);
    const float mean = rbf(row ? m1 : m0);
    const float sd = rbf(__builtin_sqrtf(rbf(rbf(row ? v1 : v0) + 1e-5f)));
    const float rc = __builtin_amdgcn_rcpf(sd);
#pragma unroll
    for (int k = 0; k < 4; ++k)
#pragma unroll
        for (int e = 0; e < 2; ++e) {
            const unsigned d = pk_bf16(bf_lo(xq[k][e]) - mean, bf_hi(xq[k][e]) - mean);
            const float n0 = bf_lo(d), n1 = bf_hi(d);
            float t0 = n0 * rc, t1 = n1 * rc;
            t0 = fmaf(fmaf(-t0, sd, n0), rc, t0);
            t1 = fmaf(fmaf(-t1, sd, n1), rc, t1);
            xp[2 * k + e] = pk_bf16(t0, t1);
        }
}

__global__ void __launch_bounds__(512, 2)
fused_mla_ffn(const u16* __restrict__ x, const u16* __restrict__ ckv, const u16* __restrict__ wq, const u16* __restrict__ wuk,
              const u16* __restrict__ wuv, const u16* __restrict__ wo, const u16* __restrict__ w1, const u16* __restrict__ b1,
              const u16* __restrict__ w2, const u16* __restrict__ b2, u16* __restrict__ out, unsigned char* ws) {
    __shared__ __attribute__((aligned(16))) unsigned char smem[LDS_BYTES];
    LAS unsigned char* L = (LAS unsigned char*)smem;
    unsigned long long stamp[NSTAMP] = {};
    STAMP(0);
    const int tid = threadIdx.x, lane = tid & 63, w = __builtin_amdgcn_readfirstlane(tid >> 6), i = lane & 15, g = lane >> 4;
    const int bid = blockIdx.x, xcd = bid & 7, idx = bid >> 3;
    const rsrc_t rsW = mkrs(ws, WS_BYTES);
    const rsrc_t rsX = mkrs(x, 2048);

    unsigned codepf = 0;
    {
        unsigned long long pc;
        asm volatile("s_getpc_b64 %0" : "=s"(pc));
        const rsrc_t rsC = mkrs((const void*)(pc & ~127ull), CODE_LINES * 128);
        codepf = __builtin_amdgcn_raw_buffer_load_b32(rsC, tid * 128, 0, 0);
    }
    const v4u xr0 = __builtin_amdgcn_raw_buffer_load_b128(rsX, lane * 16, 0, 0);
    const v4u xr1 = __builtin_amdgcn_raw_buffer_load_b128(rsX, lane * 16 + 1024, 0, 0);

    if (idx < 16) {
        const int h = 4 * xcd + (idx >> 2), cq = idx & 3, fb = h * 4 + cq;
        const int blk = WS_BLK + fb * BLK_BYTES;
        v2u xq[4];
#pragma unroll
        for (int k = 0; k < 4; ++k)
            xq[k] = __builtin_amdgcn_raw_buffer_load_b64(rsX, (i >> 3) * 1024 + (64 * w + 16 * g) * 2, ((k + cq) & 3) * 8, 0);
        const rsrc_t rsQ = mkrs(wq, 512 * 8192);
        const int qoff = (64 * w + 16 * g) * 8192 + h * 256 + i * 16;
        v4u a[16];
#pragma unroll
        for (int u = 0; u < 8; ++u) a[u] = ld128(rsQ, qoff, ((u + 4 * cq) & 15) * 8192, 0);
        __builtin_amdgcn_sched_barrier(0);
        unsigned xp[8];
        layernorm16(xr0, xr1, xq, i >> 3, xp);
        asm volatile("" ::"v"(codepf));
        __builtin_amdgcn_sched_barrier(0);
        STAMP(1);
#pragma unroll
        for (int k = 0; k < 3; ++k)
            __builtin_amdgcn_raw_buffer_store_b128(v4u{~0u, ~0u, ~0u, ~0u}, rsW, tid * 16 + k * 8192, blk, AUX_SC1);
#pragma unroll
        for (int u = 8; u < 16; ++u) a[u] = ld128(rsQ, qoff, ((u + 4 * cq) & 15) * 8192, 0);
        const rsrc_t rsK = mkrs(wuk, 512 * 8192);
        v4u uk[4];
#pragma unroll
        for (int s = 0; s < 4; ++s) uk[s] = ld128(rsK, (16 * w + i) * 8192 + g * 16 + s * 64, cq * 128 * 8192 + h * 256, 0);
        const unsigned tag = __builtin_amdgcn_raw_buffer_load_b32(rsW, WS_EPOCH, 0, AUX_SC1) + 1u;
        __builtin_amdgcn_sched_barrier(0);
        unsigned sel[2][4];
#pragma unroll
        for (int k = 0; k < 4; ++k) {
            const bool mine = ((i & 7) >> 1) == k;
            sel[0][k] = mine ? ((i & 1) ? 0x01000c0cu : 0x0c0c0100u) : 0x0c0c0c0cu;
            sel[1][k] = mine ? ((i & 1) ? 0x03020c0cu : 0x0c0c0302u) : 0x0c0c0c0cu;
        }
        f32x4 acc = {0.f, 0.f, 0.f, 0.f};
#pragma unroll
        for (int u = 0; u < 16; ++u) {
            v4u af;
#pragma unroll
            for (int k = 0; k < 4; ++k) af[k] = __builtin_amdgcn_perm(xp[u >> 1], xp[u >> 1], sel[u & 1][k]);
            acc = mfma(af, a[u], acc);
        }
        constexpr int S_PART = 131072, S_QB = S_PART + 8192;
        *(LAS f32x4*)(L + S_PART + (w * 256 + (g >> 1) * 128 + 8 * i + 4 * (g & 1)) * 4) = acc;
        VMCNT(0);
        STAMP(2);
        BARRIER();
        if (tid < 128) {
            f32x2 s = {0.f, 0.f};
#pragma unroll
            for (int ww = 0; ww < 8; ++ww) s += *(LAS f32x2*)(L + S_PART + ww * 1024 + tid * 8);
            *(LAS unsigned*)(L + S_QB + tid * 4) = pk_bf16(s.x, s.y);
        }
        BARRIER();
        {
            f32x4 d = {0.f, 0.f, 0.f, 0.f};
#pragma unroll
            for (int s = 0; s < 4; ++s) d = mfma(uk[s], *(LAS v4u*)(L + S_QB + (i & 1) * 256 + s * 64 + g * 16), d);
            if (i < 2) {
                const int P = ((2 * w + (g >> 1)) * 64 + 16 * cq + (h & 15)) * 2 + (g & 1);
                __builtin_amdgcn_raw_buffer_store_b128(v4u{pk_bf16(d.x, d.y), tag, pk_bf16(d.z, d.w), tag}, rsW,
                                                       WS_QG + ((i * 2 + (h >> 4)) * 2048 + P) * 16, 0, AUX_SC1);
            }
        }
        STAMP(3);
        BARRIER();
        constexpr int S_UV = 131072;
        {
            const unsigned char* base = (const unsigned char*)wuv + (size_t)(cq * 128 + 16 * w) * 8192 + h * 256;
#pragma unroll
            for (int j = 0; j < 4; ++j) {
                const int row = 4 * j + g;
                dma16(base, row * 8192 + ((i ^ ((row & 7) << 1)) * 16), S_UV + (16 * w + 4 * j) * 256);
            }
            const unsigned char* bo = (const unsigned char*)wo + (size_t)h * 131072 + w * 128;
            const int r8 = lane >> 3;
            const unsigned vo = r8 * 1024 + (((lane & 7) ^ (((r8 >> 1) & 3) << 1)) * 16);
#pragma unroll
            for (int j = 0; j < 16; ++j) {
                const int rg = (j + 4 * cq) & 15;
                dma16(bo + rg * 8192, vo, w * 16384 + rg * 1024);
            }
        }
        const int q4 = (lane >> 2) & 3, p4 = lane & 3, trow = 4 * g + q4;
        v4u fuv[4], fo[4][4];
        VMCNT(16);
        BARRIER();
#pragma unroll
        for (int s = 0; s < 4; ++s) {
            const int o = S_UV + (32 * s + trow) * 256 + (((2 * w + (p4 >> 1)) ^ ((trow & 7) << 1)) * 16) + 8 * (p4 & 1);
            fuv[s] = tr_frag(L, o, o + 16 * 256);
        }
#pragma unroll
        for (int s = 0; s < 4; ++s) {
            if (s == 0) VMCNT(12); else if (s == 1) VMCNT(8); else if (s == 2) VMCNT(4); else VMCNT(0);
            const int sb = w * 16384 + (((s + cq) & 3) * 32 + trow) * 128 + 8 * (p4 & 1);
#pragma unroll
            for (int T = 0; T < 4; ++T) {
                const int o = sb + (((2 * T + (p4 >> 1)) ^ (((trow >> 1) & 3) << 1)) * 16);
                fo[s][T] = tr_frag(L, o, o + 16 * 128);
            }
        }
        BARRIER();
        STAMP(4);
        v4u pr0, pr1;
        unsigned la, lo;
        for (unsigned it = 0;; ++it) {
            pr0 = ld128(rsW, w * 1024 + lane * 16, blk, 1);
            pr1 = ld128(rsW, w * 1024 + lane * 16 + 8192, blk, 1);
            la = __builtin_amdgcn_raw_buffer_load_b32(rsW, BLK_LSE + lane * 4, blk, AUX_SC1);
            lo = __builtin_amdgcn_raw_buffer_load_b32(rsW, BLK_LSE + (16 * g + (i & 8) + w) * 4, blk, AUX_SC1);
            unsigned mx = max(max(max(pr0.x, pr0.y), max(pr0.z, pr0.w)), max(max(pr1.x, pr1.y), max(pr1.z, pr1.w)));
            mx = max(mx, max(la, lo));
            if (__all(mx != ~0u) || it > SPIN_CAP) break;
            asm volatile("" ::: "memory");
        }
        STAMP(5);
        const float Mx = max_x32(max_x16(max8(bitsf(la))));
        const float wgt = __builtin_amdgcn_exp2f(bitsf(lo) - Mx);
        constexpr int S_CC = 131072, CC_ROW = 272, S_CTX = S_CC + 16 * CC_ROW;
        {
            const unsigned wp = pk_bf16(wgt, wgt);
            v4u af;
#pragma unroll
            for (int k = 0; k < 4; ++k) af[k] = __builtin_amdgcn_perm(wp, wp, sel[0][k]);
            const v4u z = {0u, 0u, 0u, 0u};
            f32x4 d = {0.f, 0.f, 0.f, 0.f};
            d = mfma(i < 8 ? af : z, pr0, d);
            d = mfma(i < 8 ? z : af, pr1, d);
            *(LAS v2u*)(L + S_CC + (8 * (g >> 1) + w) * CC_ROW + (8 * i + 4 * (g & 1)) * 2) = v2u{pk_bf16(d.x, d.y), pk_bf16(d.z, d.w)};
        }
        const float Lsum = sum_x32(sum_x16(sum8(__builtin_amdgcn_exp2f(bitsf(la) - Mx))));
        const float rL = 1.0f / Lsum;
        BARRIER();
        {
            f32x4 d = {0.f, 0.f, 0.f, 0.f};
#pragma unroll
            for (int s = 0; s < 4; ++s) d = mfma(fuv[s], *(LAS v4u*)(L + S_CC + i * CC_ROW + s * 64 + g * 16), d);
            d.x = sum8(d.x) * rL; d.y = sum8(d.y) * rL; d.z = sum8(d.z) * rL; d.w = sum8(d.w) * rL;
            if ((i & 7) == 0)
                *(LAS v2u*)(L + S_CTX + (i >> 3) * 256 + (32 * (w >> 1) + 8 * g + 4 * (w & 1)) * 2) = v2u{pk_bf16(d.x, d.y), pk_bf16(d.z, d.w)};
        }
        BARRIER();
        {
            v4u cf[4];
#pragma unroll
            for (int s = 0; s < 4; ++s) cf[s] = *(LAS v4u*)(L + S_CTX + (i & 1) * 256 + ((s + cq) & 3) * 64 + g * 16);
#pragma unroll
            for (int T = 0; T < 4; ++T) {
                f32x4 d = {0.f, 0.f, 0.f, 0.f};
#pragma unroll
                for (int s = 0; s < 4; ++s) d = mfma(fo[s][T], cf[s], d);
                if (i < 2) {
                    const int slot = i * 128 + 16 * w + 4 * T + g;
                    __builtin_amdgcn_raw_buffer_store_b128(v4u{nosent(fbits(d.x)), nosent(fbits(d.y)), nosent(fbits(d.z)), nosent(fbits(d.w))}, rsW,
                                                           WS_BLK + (slot >> 1) * BLK_BYTES + BLK_SLOT + (slot & 1) * SLOT_BYTES, fb * 16, AUX_SC1);
                }
            }
        }
        STAMP(6);
    } else {
        const int hh = idx & 1, spa = xcd * 8 + ((idx - 16) >> 1), b = spa >> 5, sp = spa & 31, aidx = spa * 2 + hh;
        v2u xq[4];
#pragma unroll
        for (int k = 0; k < 4; ++k) xq[k] = __builtin_amdgcn_raw_buffer_load_b64(rsX, (i & 1) * 1024 + (64 * w + 4 * g) * 2 + k * 32, 0, 0);
        const unsigned tag = __builtin_amdgcn_raw_buffer_load_b32(rsW, WS_EPOCH, 0, AUX_SC1) + 1u;
        unsigned xp[8];
        layernorm16(xr0, xr1, xq, i & 1, xp);
        asm volatile("" ::"v"(codepf), "v"(tag));
        __builtin_amdgcn_sched_barrier(0);
        if (A_DELAY) __builtin_amdgcn_s_sleep(A_DELAY);
        STAMP(1);
        {
            const unsigned char* base = (const unsigned char*)ckv + ((size_t)spa * 128 + 16 * w) * 1024;
#pragma unroll
            for (int j = 0; j < 16; ++j) {
                const int r = (j + 8 * hh) & 15;
                const int X = ((r & 7) << 1) | (r >> 3);
                dma16(base + r * 1024, (lane ^ X) * 16, (16 * w + r) * 1024);
            }
        }
        const int q4 = (lane >> 2) & 3, p4 = lane & 3, trow = 4 * g + q4;
        v4u kf[16], vf[4][4];
        VMCNT(0);
        STAMP(2);
        {
            const int X = ((i & 7) << 1) | (i >> 3);
#pragma unroll
            for (int s = 0; s < 16; ++s) kf[s] = *(LAS v4u*)(L + (16 * w + i) * 1024 + g * 256 + ((s ^ X) * 16));
        }
        BARRIER();
        {
            const int X = ((trow & 7) << 1) | (trow >> 3);
#pragma unroll
            for (int T = 0; T < 4; ++T) {
                const int o = trow * 1024 + (w >> 1) * 256 + (((8 * (w & 1) + 2 * T + (p4 >> 1)) ^ X) * 16) + 8 * (p4 & 1);
#pragma unroll
                for (int s = 0; s < 4; ++s) vf[s][T] = tr_frag(L, o + s * 32768, o + s * 32768 + 16384);
            }
        }
        asm volatile("s_waitcnt lgkmcnt(0)" ::: "memory");
        STAMP(3);
        constexpr int S_Q = 131072, S_P = S_Q + 16384, S_M = S_P + 4096, S_L = S_M + 512;
        {
            v4u qg[4];
            const int qb = WS_QG + (b * 2 + hh) * 32768 + w * 4096;
            const int tb = WS_QG + (b * 2 + (hh ^ 1)) * 32768 + w * 256;
            for (unsigned it = 0;; ++it) {
#pragma unroll
                for (int t = 0; t < 4; ++t) qg[t] = ld128(rsW, lane * 16 + t * 1024, qb, 1);
                const v2u tk = __builtin_amdgcn_raw_buffer_load_b64(rsW, (lane & 7) * 32, tb, AUX_SC1);
                unsigned bad = tk.y ^ tag;
#pragma unroll
                for (int t = 0; t < 4; ++t) bad |= (qg[t].y ^ tag) | (qg[t].w ^ tag);
                if (__all(bad == 0u) || it > SPIN_CAP) break;
                asm volatile("" ::: "memory");
            }
#pragma unroll
            for (int t = 0; t < 4; ++t) *(LAS v2u*)(L + S_Q + w * 2048 + t * 512 + lane * 8) = v2u{qg[t].x, qg[t].z};
        }
        STAMP(4);
        BARRIER();
        const bool ffn = aidx < NFFN;
        v4u w2r[4];
        unsigned b1v = 0;
        if (ffn) {
            const unsigned char* base = (const unsigned char*)w1 + (size_t)(64 * w) * 2752 + aidx * 32;
            dma16(base, (lane >> 1) * 2752 + (lane & 1) * 16, w * 2048);
            dma16(base + 32 * 2752, (lane >> 1) * 2752 + (lane & 1) * 16, w * 2048 + 1024);
        }
        {
            f32x4 d = {0.f, 0.f, 0.f, 0.f};
            v4u qf[16];
#pragma unroll
            for (int s = 0; s < 16; ++s) qf[s] = *(LAS v4u*)(L + S_Q + s * 1024 + lane * 16);
            __builtin_amdgcn_sched_barrier(0);
#pragma unroll
            for (int s = 0; s < 16; ++s) d = mfma(kf[s], qf[s], d);
            const float SC = 0.08838834764831845f, LOG2E = 1.4426950408889634f;
            const unsigned r0 = pk_bf16(d.x, d.y), r1 = pk_bf16(d.z, d.w);
            const unsigned s0 = pk_bf16(bf_lo(r0) * SC, bf_hi(r0) * SC), s1 = pk_bf16(bf_lo(r1) * SC, bf_hi(r1) * SC);
            const float t0 = bf_lo(s0), t1 = bf_hi(s0), t2 = bf_lo(s1), t3 = bf_hi(s1);
            const float mt = max_x32(max_x16(fmaxf(fmaxf(t0, t1), fmaxf(t2, t3))));
            const float me = __builtin_ceilf(mt * LOG2E);
            const float p0 = __builtin_amdgcn_exp2f(fmaf(t0, LOG2E, -me)), p1 = __builtin_amdgcn_exp2f(fmaf(t1, LOG2E, -me));
            const float p2 = __builtin_amdgcn_exp2f(fmaf(t2, LOG2E, -me)), p3 = __builtin_amdgcn_exp2f(fmaf(t3, LOG2E, -me));
            *(LAS v2u*)(L + S_P + (w >> 1) * 1024 + lane * 16 + (w & 1) * 8) = v2u{pk_bf16(p0, p1), pk_bf16(p2, p3)};
            const float ls = sum_x32(sum_x16((p0 + p1) + (p2 + p3)));
            if (g == 0) { *(LAS float*)(L + S_M + i * 32 + w * 4) = me; *(LAS float*)(L + S_L + i * 32 + w * 4) = ls; }
        }
        BARRIER();
        STAMP(5);
        {
            const f32x4 ma = *(LAS f32x4*)(L + S_M + i * 32), mb = *(LAS f32x4*)(L + S_M + i * 32 + 16);
            v4u pf[4];
#pragma unroll
            for (int s = 0; s < 4; ++s) pf[s] = *(LAS v4u*)(L + S_P + s * 1024 + lane * 16);
            const f32x4 la = *(LAS f32x4*)(L + S_L + i * 32), lb = *(LAS f32x4*)(L + S_L + i * 32 + 16);
            const float Mx = fmaxf(fmaxf(fmaxf(ma.x, ma.y), fmaxf(ma.z, ma.w)), fmaxf(fmaxf(mb.x, mb.y), fmaxf(mb.z, mb.w)));
            const float me[8] = {ma.x, ma.y, ma.z, ma.w, mb.x, mb.y, mb.z, mb.w};
            const float ls[8] = {la.x, la.y, la.z, la.w, lb.x, lb.y, lb.z, lb.w};
            float lsum = 0.f;
#pragma unroll
            for (int t = 0; t < 8; ++t) {
                const float df = fminf(Mx - me[t], 255.0f);
                const unsigned e = (unsigned)df * 0x00800080u;
                pf[t >> 1][2 * (t & 1)] = pk_sub_sat(pf[t >> 1][2 * (t & 1)], e);
                pf[t >> 1][2 * (t & 1) + 1] = pk_sub_sat(pf[t >> 1][2 * (t & 1) + 1], e);
                lsum = fmaf(ls[t], __builtin_amdgcn_exp2f(-df), lsum);
            }
            const float rl = 1.0f / lsum;
            const int rec = WS_BLK + ((16 * hh + i) * 4 + (w >> 1)) * BLK_BYTES + (8 * (w & 1) + g) * 16;
#pragma unroll
            for (int tp = 0; tp < 2; ++tp) {
                f32x4 d0 = {0.f, 0.f, 0.f, 0.f}, d1 = {0.f, 0.f, 0.f, 0.f};
#pragma unroll
                for (int s = 0; s < 4; ++s) { d0 = mfma(vf[s][2 * tp], pf[s], d0); d1 = mfma(vf[s][2 * tp + 1], pf[s], d1); }
                d0 *= rl; d1 *= rl;
                __builtin_amdgcn_raw_buffer_store_b128(v4u{nosent(pk_bf16(d0.x, d0.y)), nosent(pk_bf16(d0.z, d0.w)), nosent(pk_bf16(d1.x, d1.y)), nosent(pk_bf16(d1.z, d1.w))},
                                                       rsW, rec + tp * 64, spa * 256, AUX_SC1);
            }
            if (w == 0)
                __builtin_amdgcn_raw_buffer_store_b32(nosent(fbits(Mx + __builtin_amdgcn_logf(lsum))), rsW, WS_BLK + ((16 * hh + i) * 4 + g) * BLK_BYTES + BLK_LSE,
                                                      (16 * (sp & 3) + 8 * b + (sp >> 2)) * 4, AUX_SC1);
        }
        STAMP(6);
        if (ffn) {
            constexpr int S_HP = 16384, S_H = S_HP + 1024;
            if (w < 4) {
                const rsrc_t rs2 = mkrs(w2, 1376 * 1024);
#pragma unroll
                for (int u = 0; u < 4; ++u) w2r[u] = ld128(rs2, (4 * g + u) * 1024 + i * 16, aidx * 16384 + w * 256, 0);
            }
            if (tid < 32) b1v = b1[aidx * 16 + (tid & 15)];
            VMCNT(0);
            f32x4 d = {0.f, 0.f, 0.f, 0.f};
#pragma unroll
            for (int s = 0; s < 2; ++s) {
                const int o = w * 2048 + s * 1024 + trow * 32 + p4 * 8;
                d = mfma(tr_frag(L, o, o + 512), v4u{xp[4 * s], xp[4 * s + 1], xp[4 * s + 2], xp[4 * s + 3]}, d);
            }
            if (i < 2) *(LAS f32x4*)(L + S_HP + w * 128 + i * 64 + g * 16) = d;
            BARRIER();
            if (tid < 32) {
                float s = 0.f;
#pragma unroll
                for (int ww = 0; ww < 8; ++ww) s += *(LAS float*)(L + S_HP + ww * 128 + tid * 4);
                const float hv = rbf(rbf(s) + bf_lo(b1v));
                const float u2 = 2.0f * 0.7978845608028654f * 1.4426950408889634f * (hv + 0.044715f * hv * hv * hv);
                const float th = 1.0f - 2.0f / (__builtin_amdgcn_exp2f(u2) + 1.0f);
                *(LAS u16*)(L + S_H + tid * 2) = (u16)pk_bf16(0.5f * hv * (1.0f + th), 0.f);
            }
            BARRIER();
            if (w < 4) {
                const v2u hp = *(LAS v2u*)(L + S_H + (i >> 3) * 32 + g * 8);
                f32x4 y = {0.f, 0.f, 0.f, 0.f};
#pragma unroll
                for (int u = 0; u < 4; ++u) {
                    v4u af;
#pragma unroll
                    for (int k = 0; k < 4; ++k) {
                        const bool mine = ((i & 7) >> 1) == k;
                        const unsigned sl = (u & 1) ? (mine ? ((i & 1) ? 0x03020c0cu : 0x0c0c0302u) : 0x0c0c0c0cu) : (mine ? ((i & 1) ? 0x01000c0cu : 0x0c0c0100u) : 0x0c0c0c0cu);
                        af[k] = __builtin_amdgcn_perm(hp[u >> 1], hp[u >> 1], sl);
                    }
                    y = mfma(af, w2r[u], y);
                }
                const int slot = (g >> 1) * 128 + 32 * w + 2 * i + (g & 1);
                __builtin_amdgcn_raw_buffer_store_b128(v4u{nosent(fbits(y.x)), nosent(fbits(y.y)), nosent(fbits(y.z)), nosent(fbits(y.w))}, rsW,
                                                       WS_BLK + (slot >> 1) * BLK_BYTES + BLK_SLOT + (slot & 1) * SLOT_BYTES, (128 + aidx) * 16, AUX_SC1);
            }
        }
        STAMP(7);
        if (w < 2) {
            const int slot = 2 * aidx + w;
            const int sb = WS_BLK + aidx * BLK_BYTES + BLK_SLOT + w * SLOT_BYTES;
            const v2u xo = __builtin_amdgcn_raw_buffer_load_b64(rsX, slot * 8, 0, 0);
            const v2u bo = *(const v2u*)(b2 + (slot & 127) * 4);
            v4u r0, r1;
            const int l2 = lane < NFFN - 64 ? lane : 0;
            for (unsigned it = 0;; ++it) {
                r0 = ld128(rsW, 2048 + lane * 16, sb, 1);
                r1 = ld128(rsW, 3072 + l2 * 16, sb, 1);
                const unsigned mx = max(max(max(r0.x, r0.y), max(r0.z, r0.w)), max(max(r1.x, r1.y), max(r1.z, r1.w)));
                if (__all(mx != ~0u) || it > SPIN_CAP) break;
                asm volatile("" ::: "memory");
            }
            float gt[4];
#pragma unroll
            for (int e = 0; e < 4; ++e) {
                const float ys = sum64(bitsf(r0[e]) + (lane < NFFN - 64 ? bitsf(r1[e]) : 0.f));
                const float bias = (e & 1) ? bf_hi(bo[e >> 1]) : bf_lo(bo[e >> 1]);
                const float y = rbf(rbf(ys) + bias);
                gt[e] = rbf(y / (1.0f + __builtin_amdgcn_exp2f(-1.4426950408889634f * y)));
            }
            STAMP(8);
            for (unsigned it = 0;; ++it) {
                r0 = ld128(rsW, lane * 16, sb, 1);
                r1 = ld128(rsW, 1024 + lane * 16, sb, 1);
                const unsigned mx = max(max(max(r0.x, r0.y), max(r0.z, r0.w)), max(max(r1.x, r1.y), max(r1.z, r1.w)));
                if (__all(mx != ~0u) || it > SPIN_CAP) break;
                asm volatile("" ::: "memory");
            }
            STAMP(9);
            float o[4];
#pragma unroll
            for (int e = 0; e < 4; ++e) {
                const float s = rbf(sum64(bitsf(r0[e]) + bitsf(r1[e])));
                const float xv = (e & 1) ? bf_hi(xo[e >> 1]) : bf_lo(xo[e >> 1]);
                o[e] = xv + rbf(s * gt[e]);
            }
            if (lane == 0) {
                *(v2u*)(out + slot * 4) = v2u{pk_bf16(o[0], o[1]), pk_bf16(o[2], o[3])};
                if (slot == 0) __builtin_amdgcn_raw_buffer_store_b32(tag, rsW, WS_EPOCH, 0, AUX_SC1);
            }
            STAMP(10);
        }
    }
    {
        unsigned long long until = stamp[0] + 6000ull;
        if (PROBE_K > 0 && (idx < 16 ? 0 : 1) == PROBE_ROLE && stamp[PROBE_K > 0 ? PROBE_K : 0] != 0ull) until += stamp[PROBE_K > 0 ? PROBE_K : 0] - stamp[0];
        while (__builtin_amdgcn_s_memrealtime() < until) __builtin_amdgcn_s_sleep(4);
    }
}

extern "C" void kernel_launch(void* const* d_in, const int* in_sizes, int n_in, void* d_out, int out_size, void* d_ws, size_t ws_size,
                              hipStream_t stream) {
    fused_mla_ffn<<<256, 512, 0, stream>>>((const u16*)d_in[0], (const u16*)d_in[1], (const u16*)d_in[2], (const u16*)d_in[3], (const u16*)d_in[4],
                                           (const u16*)d_in[5], (const u16*)d_in[7], (const u16*)d_in[8], (const u16*)d_in[9], (const u16*)d_in[10],
                                           (u16*)d_out, (unsigned char*)d_ws);
}
```

```cpp
#include <hip/hip_runtime.h>
#include <stdint.h>

typedef unsigned short u16;
typedef unsigned v4u __attribute__((ext_vector_type(4)));
typedef unsigned v2u __attribute__((ext_vector_type(2)));
typedef float f32x4 __attribute__((ext_vector_type(4)));
typedef float f32x2 __attribute__((ext_vector_type(2)));
typedef __bf16 bf16x8 __attribute__((ext_vector_type(8)));
typedef __bf16 bf16x2 __attribute__((ext_vector_type(2)));
typedef short s16x4 __attribute__((ext_vector_type(4)));
typedef unsigned short u16x2 __attribute__((ext_vector_type(2)));
#define LAS __attribute__((address_space(3)))
typedef __amdgpu_buffer_rsrc_t rsrc_t;

constexpr int AUX_NT = 2, AUX_SC1 = 16;
constexpr int NFFN = 86;
constexpr int WS_EPOCH = 0;
constexpr int WS_QG = 4096;
constexpr int WS_Q1 = WS_QG + 64 * 2048;
constexpr int WS_H1 = WS_Q1 + 256 * 2048;
constexpr int WS_OB = WS_H1 + 88 * 2048;
constexpr int WS_ST = WS_OB + 128 * 16384;
constexpr int WS_R5 = WS_ST + 128 * 256;
constexpr int R5_CONS = 216 * 128;
constexpr int WS_BYTES = WS_R5 + 64 * R5_CONS;
constexpr unsigned SPIN_CAP = 400000u;
constexpr int LDS_BYTES = 163840;
#define CODE_LINES 132
#define A_DELAY 24
#define B_DELAY 0
#define UK_DELAY 3
#define E1_DELAY 30
#define ST_SLEEP 2
#define E2_SLEEP 2
#define FIN_DELAY 60
#define POISON_AT 0
#define A_START 110
#define E2_START 440
#define FIN_START 270
#define H1X 2

#define STAMP(k) do { } while (0)
#define CSTAMP(k) do { } while (0)
#define FINISH() do { } while (0)

__device__ __forceinline__ f32x4 mfma(v4u a, v4u b, f32x4 c) {
    return __builtin_amdgcn_mfma_f32_16x16x32_bf16(__builtin_bit_cast(bf16x8, a), __builtin_bit_cast(bf16x8, b), c, 0, 0, 0);
}
__device__ __forceinline__ unsigned pk_bf16(float lo, float hi) {
    f32x2 p = {lo, hi};
    return __builtin_bit_cast(unsigned, __builtin_convertvector(p, bf16x2));
}
__device__ __forceinline__ float bf_lo(unsigned u) { return __builtin_bit_cast(float, u << 16); }
__device__ __forceinline__ float bf_hi(unsigned u) { return __builtin_bit_cast(float, u & 0xffff0000u); }
__device__ __forceinline__ float rbf(float x) { return bf_lo(pk_bf16(x, x)); }
__device__ __forceinline__ unsigned nosent(unsigned u) { return u < 0xfffffffeu ? u : 0xfffffffeu; }
__device__ __forceinline__ unsigned fbits(float f) { return __builtin_bit_cast(unsigned, f); }
__device__ __forceinline__ float bitsf(unsigned u) { return __builtin_bit_cast(float, u); }
__device__ __forceinline__ unsigned pk_sub_sat(unsigned a, unsigned b) {
    return __builtin_bit_cast(unsigned, __builtin_elementwise_sub_sat(__builtin_bit_cast(u16x2, a), __builtin_bit_cast(u16x2, b)));
}
__device__ __forceinline__ float vmax(float a, float b) { float r; asm("v_max_f32 %0, %1, %2" : "=v"(r) : "v"(a), "v"(b)); return r; }
__device__ __forceinline__ float vmax3(float a, float b, float c) { float r; asm("v_max3_f32 %0, %1, %2, %3" : "=v"(r) : "v"(a), "v"(b), "v"(c)); return r; }
template <int CTRL> __device__ __forceinline__ float dpp(float v) {
    return __builtin_bit_cast(float, __builtin_amdgcn_update_dpp(0, __builtin_bit_cast(int, v), CTRL, 0xf, 0xf, true));
}
constexpr int DPP_X1 = 0xB1, DPP_X2 = 0x4E, DPP_HMIR = 0x141, DPP_MIR = 0x140, DPP_ROR8 = 0x128;
__device__ __forceinline__ float sum_x16(float v) { auto r = __builtin_amdgcn_permlane16_swap(fbits(v), fbits(v), false, false); return bitsf(r[0]) + bitsf(r[1]); }
__device__ __forceinline__ float sum_x32(float v) { auto r = __builtin_amdgcn_permlane32_swap(fbits(v), fbits(v), false, false); return bitsf(r[0]) + bitsf(r[1]); }
__device__ __forceinline__ float max_x16(float v) { auto r = __builtin_amdgcn_permlane16_swap(fbits(v), fbits(v), false, false); return vmax(bitsf(r[0]), bitsf(r[1])); }
__device__ __forceinline__ float max_x32(float v) { auto r = __builtin_amdgcn_permlane32_swap(fbits(v), fbits(v), false, false); return vmax(bitsf(r[0]), bitsf(r[1])); }
__device__ __forceinline__ float sum8(float v) { v += dpp<DPP_X1>(v); v += dpp<DPP_X2>(v); v += dpp<DPP_HMIR>(v); return v; }
__device__ __forceinline__ float max8(float v) { v = vmax(v, dpp<DPP_X1>(v)); v = vmax(v, dpp<DPP_X2>(v)); v = vmax(v, dpp<DPP_HMIR>(v)); return v; }
__device__ __forceinline__ float sum64(float v) {
    v += dpp<DPP_X1>(v); v += dpp<DPP_X2>(v); v += dpp<DPP_HMIR>(v); v += dpp<DPP_MIR>(v);
    return sum_x32(sum_x16(v));
}

__device__ __forceinline__ rsrc_t mkrs(const void* p, int bytes) { return __builtin_amdgcn_make_buffer_rsrc((void*)p, 0, bytes, 0x00020000); }
__device__ __forceinline__ v4u ld_nt(rsrc_t r, int voff, int soff) { return __builtin_amdgcn_raw_buffer_load_b128(r, voff, soff, AUX_NT); }
__device__ __forceinline__ v4u ld_dev(rsrc_t r, int voff, int soff) { return __builtin_amdgcn_raw_buffer_load_b128(r, voff, soff, AUX_SC1); }
__device__ __forceinline__ void st_dev(v4u v, rsrc_t r, int voff, int soff) { __builtin_amdgcn_raw_buffer_store_b128(v, r, voff, soff, AUX_SC1); }
__device__ __forceinline__ void dma16(const void* sbase, unsigned voff, unsigned ldsdst) {
    asm volatile("s_mov_b32 m0, %2\n\ts_nop 0\n\tglobal_load_lds_dwordx4 %0, %1 nt" ::"v"(voff), "s"(sbase), "s"(ldsdst) : "memory");
}
#define VMCNT(n) asm volatile("s_waitcnt vmcnt(" #n ")" ::: "memory")
#define BARRIER() do { asm volatile("s_waitcnt lgkmcnt(0)" ::: "memory"); __builtin_amdgcn_s_barrier(); asm volatile("" ::: "memory"); } while (0)
#define PIN(x) asm volatile("" : "+v"(x))
#define PIN2() asm volatile("" ::: "memory")
#define SCHED() __builtin_amdgcn_sched_barrier(0)

__device__ __forceinline__ v4u tr_frag(LAS unsigned char* L, int off0, int off1) {
    s16x4 a = __builtin_amdgcn_ds_read_tr16_b64_v4i16((LAS s16x4*)(L + off0));
    s16x4 b = __builtin_amdgcn_ds_read_tr16_b64_v4i16((LAS s16x4*)(L + off1));
    v2u ua = __builtin_bit_cast(v2u, a), ub = __builtin_bit_cast(v2u, b);
    return v4u{ua.x, ua.y, ub.x, ub.y};
}

__device__ __forceinline__ void ln_stats(v4u xr0, v4u xr1, int row, float& mean, float& sd) {
    const float p0 = bf_lo(__builtin_amdgcn_readfirstlane(xr0.x)), p1 = bf_lo(__builtin_amdgcn_readfirstlane(xr1.x));
    float s0 = 0.f, s1 = 0.f, q0 = 0.f, q1 = 0.f;
#pragma unroll
    for (int e = 0; e < 4; ++e) {
        const float a0 = bf_lo(xr0[e]) - p0, a1 = bf_hi(xr0[e]) - p0, b0 = bf_lo(xr1[e]) - p1, b1 = bf_hi(xr1[e]) - p1;
        s0 += a0 + a1; s1 += b0 + b1;
        q0 = fmaf(a0, a0, fmaf(a1, a1, q0)); q1 = fmaf(b0, b0, fmaf(b1, b1, q1));
    }
    s0 = sum64(s0); s1 = sum64(s1); q0 = sum64(q0); q1 = sum64(q1);
    const float md = (row ? s1 : s0) * (1.0f / 512.0f);
    const float var = fmaxf(fmaf(-md, md, (row ? q1 : q0) * (1.0f / 512.0f)), 0.f);
    mean = rbf(md + (row ? p1 : p0));
    sd = rbf(__builtin_sqrtf(rbf(rbf(var) + 1e-5f)));
}
__device__ __forceinline__ unsigned ln_apply(unsigned xv, float mean, float sd, float rc) {
    const unsigned d = pk_bf16(bf_lo(xv) - mean, bf_hi(xv) - mean);
    const float n0 = bf_lo(d), n1 = bf_hi(d);
    float t0 = n0 * rc, t1 = n1 * rc;
    t0 = fmaf(fmaf(-t0, sd, n0), rc, t0);
    t1 = fmaf(fmaf(-t1, sd, n1), rc, t1);
    return pk_bf16(t0, t1);
}
__device__ __forceinline__ unsigned sel_of(int i, int k, int odd) {
    const bool mine = ((i & 7) >> 1) == k;
    return mine ? ((i & 1) ? (odd ? 0x03020c0cu : 0x01000c0cu) : (odd ? 0x0c0c0302u : 0x0c0c0100u)) : 0x0c0c0c0cu;
}
__device__ __forceinline__ unsigned get_tag(unsigned epoch) { PIN(epoch); return epoch + 1u; }
__device__ __forceinline__ unsigned umax4(v4u v) { return max(max(v.x, v.y), max(v.z, v.w)); }
__device__ __forceinline__ unsigned badtag(v4u v, unsigned tag) { return (v.y ^ tag) | (v.w ^ tag); }

__global__ void __launch_bounds__(512, 2)
fused_mla_ffn(const u16* __restrict__ x, const u16* __restrict__ ckv, const u16* __restrict__ wq, const u16* __restrict__ wuk,
              const u16* __restrict__ wuv, const u16* __restrict__ wo, const u16* __restrict__ w1, const u16* __restrict__ b1,
              const u16* __restrict__ w2, const u16* __restrict__ b2, u16* __restrict__ out, unsigned char* ws) {
    __shared__ __attribute__((aligned(16))) unsigned char smem[LDS_BYTES];
    LAS unsigned char* L = (LAS unsigned char*)smem;
    STAMP(0);
    const unsigned long long tstart = __builtin_amdgcn_s_memrealtime();
    const int tid = threadIdx.x, lane = tid & 63, w = __builtin_amdgcn_readfirstlane(tid >> 6), i = lane & 15, g = lane >> 4;
    const int bid = blockIdx.x, xcd = bid & 7, idx = bid >> 3;
    unsigned codepf = 0;
    {
        unsigned long long pc;
        asm volatile("s_getpc_b64 %0" : "=s"(pc));
        const rsrc_t rsC = mkrs((const void*)(pc & ~127ull), CODE_LINES * 128);
        codepf = __builtin_amdgcn_raw_buffer_load_b32(rsC, (tid ^ 384) * 128, 0, 0);
    }
    SCHED();
    asm volatile("" ::"s"(x), "s"(ckv), "s"(wq), "s"(wuk), "s"(wuv), "s"(wo), "s"(w1), "s"(b1), "s"(w2), "s"(b2), "s"(out), "s"(ws));
    STAMP(11);
    const rsrc_t rsW = mkrs(ws, WS_BYTES);
    const rsrc_t rsX = mkrs(x, 2048);
    constexpr int S_UK = 98304;
    unsigned epoch = __builtin_amdgcn_raw_buffer_load_b32(rsW, WS_EPOCH, 0, AUX_SC1);
    PIN2();
    v4u xr0, xr1, xqa, xqb, a[16];
    unsigned ep1;
    if (idx < 16 && w < 2) {
        ep1 = __builtin_amdgcn_raw_buffer_load_b32(rsW, WS_EPOCH + 0 * lane, 0, AUX_SC1);
        PIN2();
        const int t8 = 2 * (idx & 3) + w, h1 = (4 * xcd + (idx >> 2)) ^ (2 + w);
        xr0 = __builtin_amdgcn_raw_buffer_load_b128(rsX, lane * 16, 0, 0);
        xr1 = __builtin_amdgcn_raw_buffer_load_b128(rsX, lane * 16 + 1024, 0, 0);
        xqa = __builtin_amdgcn_raw_buffer_load_b128(rsX, (i >> 3) * 1024 + g * 32, t8 * 128, 0);
        xqb = __builtin_amdgcn_raw_buffer_load_b128(rsX, (i >> 3) * 1024 + g * 32 + 16, t8 * 128, 0);
        const rsrc_t rsQ = mkrs(wq, 512 * 8192);
#pragma unroll
        for (int u = 0; u < 16; ++u) a[u] = ld_nt(rsQ, g * 131072 + i * 16, (64 * t8 + u) * 8192 + h1 * 256);
        SCHED();
        STAMP(1);
    }
    if (idx < 16 && w >= 4) {
        if (UK_DELAY) __builtin_amdgcn_s_sleep(UK_DELAY);
        const unsigned char* base = (const unsigned char*)wuk + (size_t)((idx & 3) * 128 + 32 * (w - 4)) * 8192 + (4 * xcd + (idx >> 2)) * 256;
#pragma unroll
        for (int j = 0; j < 8; ++j) dma16(base + j * 32768, g * 8192 + ((i ^ ((4 * j + g) & 15)) * 16), S_UK + (w - 4) * 8192 + j * 1024);
        STAMP(1);
    }
    const v4u ones = {~0u, ~0u, ~0u, ~0u};
    const int q4 = (lane >> 2) & 3, p4 = lane & 3, trow = 4 * g + q4;

    if (__builtin_expect(idx >= 16, 0)) {
        const int hh = idx & 1, spa = xcd * 8 + ((idx - 16) >> 1), b = spa >> 5, aidx = spa * 2 + hh;
        const int tw = (w + 4 * hh) & 7;
        constexpr int QF = 1040, S_Q = 131072, S_P = S_Q + 16 * QF, S_M = S_P + 4096, S_L = S_M + 512;
        asm volatile("" ::"v"(codepf));
        const unsigned tag = get_tag(epoch);
        while (__builtin_amdgcn_s_memrealtime() < tstart + A_START) __builtin_amdgcn_s_sleep(1);
        const int fidx = (aidx >> 1) + (aidx & 1) * 64;
        STAMP(1);
        {
            const unsigned char* base = (const unsigned char*)ckv + ((size_t)spa * 128 + 16 * tw) * 1024;
#pragma unroll
            for (int r = 0; r < 16; ++r) dma16(base + r * 1024, (lane ^ (((r & 7) << 1) | (r >> 3))) * 16, (16 * tw + r) * 1024);
        }
        st_dev(ones, rsW, tid * 16, WS_OB + aidx * 16384);
        st_dev(ones, rsW, tid * 16 + 8192, WS_OB + aidx * 16384);
        v4u kf[16], vf[4][4];
        VMCNT(2);
        STAMP(2);
        {
            const int X = ((i & 7) << 1) | (i >> 3);
#pragma unroll
            for (int s = 0; s < 16; ++s) kf[s] = *(LAS v4u*)(L + (16 * tw + i) * 1024 + g * 256 + ((s ^ X) * 16));
        }
        int qw = S_Q + ((lane & 31) >> 1) * QF + ((lane >> 5) * 16 + 2 * w) * 16 + (lane & 1) * 8;
        int qr = S_Q + lane * 16, pw = S_P + (tw >> 1) * 1024 + lane * 16 + (tw & 1) * 8, mw = S_M + i * 32 + tw * 4, mr = S_M + i * 32;
        int obo = (i & 7) * 128 + (i >> 3) * 64 + g * 16;
        PIN(qw); PIN(qr); PIN(pw); PIN(mw); PIN(mr); PIN(obo);
        BARRIER();
        STAMP(3);
        {
            v4u qg[4];
            const int qb = WS_QG + (b * 32 + hh * 16 + 2 * w) * 2048;
            const bool late = __builtin_amdgcn_s_memrealtime() >= tstart + E2_START;
            if (late) {
#pragma unroll
                for (int t = 0; t < 4; ++t) qg[t] = ld_dev(rsW, lane * 16 + t * 1024, qb);
            }
            SCHED();
            {
                const int X = ((trow & 7) << 1) | (trow >> 3);
#pragma unroll
                for (int T = 0; T < 4; ++T) {
                    const int o = trow * 1024 + (w >> 1) * 256 + (((8 * (w & 1) + 2 * T + (p4 >> 1)) ^ X) * 16) + 8 * (p4 & 1);
#pragma unroll
                    for (int s = 0; s < 4; ++s) vf[s][T] = tr_frag(L, o + s * 32768, o + s * 32768 + 16384);
                }
            }
            SCHED();
            if (!late) {
                while (__builtin_amdgcn_s_memrealtime() < tstart + E2_START) __builtin_amdgcn_s_sleep(1);
#pragma unroll
                for (int t = 0; t < 4; ++t) qg[t] = ld_dev(rsW, lane * 16 + t * 1024, qb);
            }
            for (unsigned it = 0;; ++it) {
                unsigned bad = 0u;
#pragma unroll
                for (int t = 0; t < 4; ++t) bad |= (qg[t].y ^ tag) | (qg[t].w ^ tag);
                if (__all(bad == 0u) || it > SPIN_CAP) break;
                asm volatile("" ::: "memory");
                __builtin_amdgcn_s_sleep(E2_SLEEP);
#pragma unroll
                for (int t = 0; t < 4; ++t)
                    if (__any(badtag(qg[t], tag) != 0u)) qg[t] = ld_dev(rsW, lane * 16 + t * 1024, qb);
            }
            VMCNT(0);
#pragma unroll
            for (int t = 0; t < 4; ++t) *(LAS v2u*)(L + qw + (t & 1) * 512 + (t >> 1) * 16) = v2u{qg[t].x, qg[t].z};
        }
        STAMP(4);
        CSTAMP(12);
        BARRIER();
        CSTAMP(13);
        float Mx;
        {
            f32x4 d = {0.f, 0.f, 0.f, 0.f};
            v4u qf[16];
#pragma unroll
            for (int s = 0; s < 16; ++s) qf[s] = *(LAS v4u*)(L + qr + s * QF);
            SCHED();
            if (fidx < NFFN) {
                const unsigned char* b2w = (const unsigned char*)w2 + (size_t)fidx * 16384 + w * 2048;
                dma16(b2w, lane * 16, w * 2048);
                dma16(b2w + 1024, lane * 16, w * 2048 + 1024);
            }
            SCHED();
#pragma unroll
            for (int s = 0; s < 16; ++s) d = mfma(kf[s], qf[s], d);
            const float SC = 0.08838834764831845f, LOG2E = 1.4426950408889634f;
            const unsigned r0 = pk_bf16(d.x, d.y), r1 = pk_bf16(d.z, d.w);
            const unsigned s0 = pk_bf16(bf_lo(r0) * SC, bf_hi(r0) * SC), s1 = pk_bf16(bf_lo(r1) * SC, bf_hi(r1) * SC);
            const float t0 = bf_lo(s0), t1 = bf_hi(s0), t2 = bf_lo(s1), t3 = bf_hi(s1);
            const float mt = max_x32(max_x16(vmax(vmax3(t0, t1, t2), t3)));
            if (g == 0) *(LAS float*)(L + mw) = mt;
            CSTAMP(14);
            BARRIER();
            const f32x4 ma = *(LAS f32x4*)(L + mr), mb = *(LAS f32x4*)(L + mr + 16);
            Mx = __builtin_ceilf(vmax3(vmax3(ma.x, ma.y, ma.z), vmax3(ma.w, mb.x, mb.y), vmax(mb.z, mb.w)) * LOG2E);
            const float p0 = __builtin_amdgcn_exp2f(fmaf(t0, LOG2E, -Mx)), p1 = __builtin_amdgcn_exp2f(fmaf(t1, LOG2E, -Mx));
            const float p2 = __builtin_amdgcn_exp2f(fmaf(t2, LOG2E, -Mx)), p3 = __builtin_amdgcn_exp2f(fmaf(t3, LOG2E, -Mx));
            *(LAS v2u*)(L + pw) = v2u{pk_bf16(p0, p1), pk_bf16(p2, p3)};
            *(LAS float*)(L + S_L + tw * 256 + lane * 4) = (p0 + p1) + (p2 + p3);
        }
        CSTAMP(15);
        BARRIER();
        STAMP(5);
        CSTAMP(16);
        {
            v4u pf[4];
#pragma unroll
            for (int s = 0; s < 4; ++s) pf[s] = *(LAS v4u*)(L + qr + (S_P - S_Q) + s * 1024);
            if (w == 4) {
                float ls = 0.f;
#pragma unroll
                for (int t = 0; t < 8; ++t) ls += *(LAS float*)(L + S_L + t * 256 + lane * 4);
                ls = sum_x32(sum_x16(ls));
                if (g == 0) st_dev(v4u{fbits(Mx), tag, fbits(ls), tag}, rsW, lane * 16, WS_ST + aidx * 256);
            }
            SCHED();
            f32x4 d[4];
#pragma unroll
            for (int T = 0; T < 4; ++T) d[T] = f32x4{0.f, 0.f, 0.f, 0.f};
#pragma unroll
            for (int s = 0; s < 4; ++s)
#pragma unroll
                for (int T = 0; T < 4; ++T) d[T] = mfma(vf[s][T], pf[s], d[T]);
            const v4u t0 = {nosent(pk_bf16(d[0].x, d[0].y)), nosent(pk_bf16(d[0].z, d[0].w)), nosent(pk_bf16(d[1].x, d[1].y)), nosent(pk_bf16(d[1].z, d[1].w))};
            const v4u t1 = {nosent(pk_bf16(d[2].x, d[2].y)), nosent(pk_bf16(d[2].z, d[2].w)), nosent(pk_bf16(d[3].x, d[3].y)), nosent(pk_bf16(d[3].z, d[3].w))};
            v4u sa, sb;
#pragma unroll
            for (int k = 0; k < 4; ++k) {
                sa[k] = (unsigned)__builtin_amdgcn_update_dpp((int)t0[k], (int)t1[k], DPP_ROR8, 0xf, 0xc, false);
                sb[k] = (unsigned)__builtin_amdgcn_update_dpp((int)t1[k], (int)t0[k], DPP_ROR8, 0xf, 0x3, false);
            }
            st_dev(sa, rsW, obo, WS_OB + aidx * 16384 + w * 2048);
            st_dev(sb, rsW, obo + 1024, WS_OB + aidx * 16384 + w * 2048);
        }
        STAMP(6);
        CSTAMP(18);
        const unsigned long long tpub = __builtin_amdgcn_s_memrealtime();
        if (fidx < NFFN) {
            constexpr int S_H = 16384;
            if (w == 0) {
                const unsigned bias = *(const unsigned*)(b1 + fidx * 16 + (lane & 1) * 8 + ((lane >> 1) & 1) * 4 + ((lane >> 3) & 1) * 2);
                const int ho = (lane >> 3) * 2048 + (((lane >> 1) & 3) * 16 + (lane & 1)) * 16, hs = WS_H1 + (fidx >> 3) * 16384 + (fidx & 7) * 32;
                v4u o0, o1;
                for (unsigned it = 0;; ++it) {
                    o0 = ld_dev(rsW, ho, hs);
                    o1 = ld_dev(rsW, ho + 1024, hs);
                    if (__all(((o0.y ^ tag) | (o0.w ^ tag) | (o1.y ^ tag) | (o1.w ^ tag)) == 0u) || it > SPIN_CAP) break;
                    asm volatile("" ::: "memory");
                }
                float v[4] = {bitsf(o0.x), bitsf(o0.z), bitsf(o1.x), bitsf(o1.z)};
#pragma unroll
                for (int e = 0; e < 4; ++e) { v[e] += dpp<DPP_ROR8>(v[e]); v[e] = sum_x32(sum_x16(v[e])); }
                float hq[2];
                const float va = (lane & 8) ? v[2] : v[0], vb = (lane & 8) ? v[3] : v[1];
#pragma unroll
                for (int e = 0; e < 2; ++e) {
                    const float hv = rbf(rbf(e ? vb : va) + (e ? bf_hi(bias) : bf_lo(bias)));
                    const float u2 = 2.0f * 0.7978845608028654f * 1.4426950408889634f * (hv + 0.044715f * hv * hv * hv);
                    const float th = 1.0f - 2.0f / (__builtin_amdgcn_exp2f(u2) + 1.0f);
                    hq[e] = 0.5f * hv * (1.0f + th);
                }
                if (lane < 16) *(LAS unsigned*)(L + S_H + ((lane >> 2) & 1) * 32 + ((lane & 1) * 8 + ((lane >> 1) & 1) * 4 + ((lane >> 3) & 1) * 2) * 2) = pk_bf16(hq[0], hq[1]);
            }
            VMCNT(2);
            BARRIER();
            if (w < 4) {
                const v2u hp = *(LAS v2u*)(L + S_H + (i >> 3) * 32 + g * 8);
                f32x4 y = {0.f, 0.f, 0.f, 0.f};
#pragma unroll
                for (int u = 0; u < 4; ++u) {
                    v4u af;
#pragma unroll
                    for (int k = 0; k < 4; ++k) af[k] = __builtin_amdgcn_perm(hp[u >> 1], hp[u >> 1], sel_of(i, k, u & 1));
                    y = mfma(af, *(LAS v4u*)(L + (4 * g + u) * 1024 + w * 256 + i * 16), y);
                }
                const int yo = ((g >> 1) * 32 + (i >> 1)) * R5_CONS + ((i & 1) * 4 + (g & 1) * 2) * 16, ys = WS_R5 + 8 * w * R5_CONS + (128 + fidx) * 128;
                st_dev(v4u{fbits(y.x), tag, fbits(y.y), tag}, rsW, yo, ys);
                st_dev(v4u{fbits(y.z), tag, fbits(y.w), tag}, rsW, yo + 16, ys);
            }
            STAMP(7);
        }
        if (aidx & 1) {
            const int c64 = aidx >> 1, rb = WS_R5 + c64 * R5_CONS;
            constexpr int S_RY = 32768, S_RS = S_RY + 512;
            const unsigned xo = x[c64 * 16 + i], bo = b2[(c64 & 31) * 16 + i];
            while (__builtin_amdgcn_s_memrealtime() < tpub + FIN_START) __builtin_amdgcn_s_sleep(2);
            v4u r0, r1, y0, y1;
            const bool has2 = 64 + 8 * w + (lane >> 3) < NFFN;
            STAMP(8);
            y0 = ld_dev(rsW, lane * 16, rb + 16384 + w * 1024);
            y1 = ld_dev(rsW, (has2 ? 8192 : 0) + lane * 16, rb + 16384 + w * 1024);
            r0 = ld_dev(rsW, lane * 16, rb + w * 2048);
            r1 = ld_dev(rsW, lane * 16 + 1024, rb + w * 2048);
            for (unsigned it = 0;; ++it) {
                if (__all((badtag(r0, tag) | badtag(r1, tag) | badtag(y0, tag) | badtag(y1, tag)) == 0u) || it > SPIN_CAP) break;
                asm volatile("" ::: "memory");
                if (__any((badtag(y0, tag) | badtag(y1, tag)) != 0u)) {
                    y0 = ld_dev(rsW, lane * 16, rb + 16384 + w * 1024);
                    y1 = ld_dev(rsW, (has2 ? 8192 : 0) + lane * 16, rb + 16384 + w * 1024);
                }
                if (__any(badtag(r0, tag) != 0u)) r0 = ld_dev(rsW, lane * 16, rb + w * 2048);
                if (__any(badtag(r1, tag) != 0u)) r1 = ld_dev(rsW, lane * 16 + 1024, rb + w * 2048);
            }
            STAMP(9);
            {
                float v[4] = {bitsf(r0.x) + bitsf(r1.x), bitsf(r0.z) + bitsf(r1.z), bitsf(y0.x) + (has2 ? bitsf(y1.x) : 0.f), bitsf(y0.z) + (has2 ? bitsf(y1.z) : 0.f)};
#pragma unroll
                for (int e = 0; e < 4; ++e) { v[e] += dpp<DPP_ROR8>(v[e]); v[e] = sum_x32(sum_x16(v[e])); }
                if (lane < 8) { *(LAS f32x2*)(L + S_RS + w * 64 + lane * 8) = f32x2{v[0], v[1]}; *(LAS f32x2*)(L + S_RY + w * 64 + lane * 8) = f32x2{v[2], v[3]}; }
            }
            BARRIER();
            if (w == 0) {
                const float s = sum_x32(sum_x16(*(LAS float*)(L + S_RS + g * 128 + i * 4) + *(LAS float*)(L + S_RS + g * 128 + 64 + i * 4)));
                const float ys = sum_x32(sum_x16(*(LAS float*)(L + S_RY + g * 128 + i * 4) + *(LAS float*)(L + S_RY + g * 128 + 64 + i * 4)));
                const float y = rbf(rbf(ys) + bf_lo(bo));
                const float gt = rbf(y * __builtin_amdgcn_rcpf(1.0f + __builtin_amdgcn_exp2f(-1.4426950408889634f * y)));
                if (g == 0) out[c64 * 16 + i] = (u16)pk_bf16(bf_lo(xo) + rbf(rbf(s) * gt), 0.f);
                if (c64 == 0 && lane == 0) __builtin_amdgcn_raw_buffer_store_b32(tag, rsW, WS_EPOCH, 0, AUX_SC1);
            }
            STAMP(10);
        }
        FINISH();
        return;
    }
    {
        const int h = 4 * xcd + (idx >> 2), cq = idx & 3, fb = h * 4 + cq;
        constexpr int S_WO = 0, S_UV = 65536, S_QB = 131072;
        v4u uk[2][4], af[16];
        const int t8 = 2 * cq + (w & 1);
        if (__builtin_expect(w < 2, 1)) {
            const int h1 = h ^ (2 + w);
            float mean, sd;
            ln_stats(xr0, xr1, i >> 3, mean, sd);
            const float rc = __builtin_amdgcn_rcpf(sd);
#pragma unroll
            for (int e = 0; e < 8; ++e) {
                const unsigned xp = ln_apply(e < 4 ? xqa[e & 3] : xqb[e & 3], mean, sd, rc);
#pragma unroll
                for (int k = 0; k < 4; ++k) { af[2 * e][k] = __builtin_amdgcn_perm(xp, xp, sel_of(i, k, 0)); af[2 * e + 1][k] = __builtin_amdgcn_perm(xp, xp, sel_of(i, k, 1)); }
            }
            const unsigned tg = get_tag(ep1);
            SCHED();
            STAMP(7);
            f32x4 acc = {0.f, 0.f, 0.f, 0.f};
#pragma unroll
            for (int u = 0; u < 16; ++u) acc = mfma(af[u], a[u], acc);
            st_dev(v4u{fbits(acc.x), tg, fbits(acc.y), tg}, rsW, lane * 16, WS_Q1 + (h1 * 8 + t8) * 2048);
            st_dev(v4u{fbits(acc.z), tg, fbits(acc.w), tg}, rsW, lane * 16 + 1024, WS_Q1 + (h1 * 8 + t8) * 2048);
            STAMP(19);
            int qo = S_QB + (g >> 1) * 256 + (8 * i + 4 * (g & 1) + 2 * w) * 2;
            PIN(qo);
            if (E1_DELAY) __builtin_amdgcn_s_sleep(E1_DELAY); else VMCNT(0);
            STAMP(17);
            v4u o[8];
#pragma unroll
            for (int k = 0; k < 8; ++k) o[k] = ld_dev(rsW, lane * 16 + (k & 1) * 2048, WS_Q1 + h * 16384 + w * 1024 + (k >> 1) * 4096);
            for (unsigned it = 0;; ++it) {
                unsigned bad = 0u;
#pragma unroll
                for (int k = 0; k < 8; ++k) bad |= badtag(o[k], tg);
                if (__all(bad == 0u) || it > SPIN_CAP) break;
                asm volatile("" ::: "memory");
#pragma unroll
                for (int k = 0; k < 8; ++k)
                    if (__any(badtag(o[k], tg) != 0u)) o[k] = ld_dev(rsW, lane * 16 + (k & 1) * 2048, WS_Q1 + h * 16384 + w * 1024 + (k >> 1) * 4096);
            }
            STAMP(9);
            *(LAS unsigned*)(L + qo) = pk_bf16(((bitsf(o[0].x) + bitsf(o[1].x)) + (bitsf(o[2].x) + bitsf(o[3].x))) + ((bitsf(o[4].x) + bitsf(o[5].x)) + (bitsf(o[6].x) + bitsf(o[7].x))),
                                               ((bitsf(o[0].z) + bitsf(o[1].z)) + (bitsf(o[2].z) + bitsf(o[3].z))) + ((bitsf(o[4].z) + bitsf(o[5].z)) + (bitsf(o[6].z) + bitsf(o[7].z))));
        } else if (w >= 4) {
            VMCNT(0);
#pragma unroll
            for (int t = 0; t < 2; ++t)
#pragma unroll
                for (int s = 0; s < 4; ++s) uk[t][s] = *(LAS v4u*)(L + S_UK + (w - 4) * 8192 + (16 * t + i) * 256 + (((4 * s + g) ^ i) * 16));
            STAMP(8);
        }
        const unsigned tag = get_tag(epoch);
        int qro = S_QB + (i & 1) * 256 + g * 16, pubo = (i & 1) * 65536 + ((i >> 1) * 4 + g) * 16;
        PIN(qro); PIN(pubo);
        BARRIER();
        if (w >= 4) {
            v4u qf[4];
#pragma unroll
            for (int s = 0; s < 4; ++s) qf[s] = *(LAS v4u*)(L + qro + s * 64);
            SCHED();
            f32x4 d0 = {0.f, 0.f, 0.f, 0.f}, d1 = {0.f, 0.f, 0.f, 0.f};
#pragma unroll
            for (int s = 0; s < 4; ++s) { d0 = mfma(uk[0][s], qf[s], d0); d1 = mfma(uk[1][s], qf[s], d1); }
            const f32x4 d = (i & 2) ? d1 : d0;
            if (i < 4)
                st_dev(v4u{pk_bf16(d.x, d.y), tag, pk_bf16(d.z, d.w), tag}, rsW, pubo, WS_QG + h * 2048 + cq * 512 + (w - 4) * 128);
        } else {
            __builtin_amdgcn_s_sleep(7);
        }
        STAMP(3);
        asm volatile("" ::"v"(codepf));
        const int ch = cq >> 1, dh = cq & 1, slab = (w + 4 * ch) & 7;
        int r5o = ((i & 1) * 32 + (i >> 2)) * R5_CONS + (2 * g + ((i >> 1) & 1)) * 16;
        const int r5s = WS_R5 + 4 * slab * R5_CONS + fb * 128;
        if (B_DELAY) __builtin_amdgcn_s_sleep(B_DELAY);
        const int cb = h / 3;
        const bool ffn1 = w < 2 && h - cb * 3 == w;
        if (ffn1) {
            const unsigned char* base = (const unsigned char*)w1 + (size_t)(64 * t8) * 2752 + cb * 256;
            const unsigned vo = g * (16 * 2752) + (cb * 256 + i * 16 < 2752 ? i * 16 : 0);
#pragma unroll
            for (int u = 0; u < 16; ++u) dma16(base + u * 2752, vo, S_UK + u * 1024);
        }
        {
            const int r8 = lane >> 3;
            const unsigned sw = ((lane & 7) ^ (((r8 >> 1) & 3) << 1)) * 16;
            const unsigned char* base = (const unsigned char*)wuv + (size_t)(ch * 256 + 32 * w) * 8192 + h * 256 + dh * 128;
#pragma unroll
            for (int j = 0; j < 4; ++j) dma16(base + j * 65536, r8 * 8192 + sw, S_UV + (32 * w + 8 * j) * 128);
            const unsigned char* bo = (const unsigned char*)wo + (size_t)h * 131072 + dh * 65536 + slab * 128;
#pragma unroll
            for (int j = 0; j < 8; ++j) dma16(bo + j * 8192, r8 * 1024 + sw, S_WO + w * 8192 + j * 1024);
        }
        if (ffn1) {
            VMCNT(12);
            f32x4 acc = {0.f, 0.f, 0.f, 0.f};
#pragma unroll
            for (int u = 0; u < 16; ++u) acc = mfma(af[u], *(LAS v4u*)(L + S_UK + u * 1024 + lane * 16), acc);
            st_dev(v4u{fbits(acc.x), tag, fbits(acc.y), tag}, rsW, lane * 16, WS_H1 + (cb * 8 + t8) * 2048);
            st_dev(v4u{fbits(acc.z), tag, fbits(acc.w), tag}, rsW, lane * 16 + 1024, WS_H1 + (cb * 8 + t8) * 2048);
            STAMP(10);
            VMCNT(2);
        } else {
            VMCNT(0);
        }
        v4u fuv[4], fo[2][4];
        const int tsw = ((trow >> 1) & 3) << 1;
#pragma unroll
        for (int s = 0; s < 2; ++s)
#pragma unroll
            for (int T = 0; T < 4; ++T) {
                const int o = S_WO + w * 8192 + (s * 32 + trow) * 128 + (((2 * T + (p4 >> 1)) ^ tsw) * 16) + 8 * (p4 & 1);
                fo[s][T] = tr_frag(L, o, o + 16 * 128);
            }
        BARRIER();
#pragma unroll
        for (int s = 0; s < 4; ++s) {
            const int o = S_UV + (128 * (w >> 2) + 32 * s + trow) * 128 + (((2 * (w & 3) + (p4 >> 1)) ^ tsw) * 16) + 8 * (p4 & 1);
            fuv[s] = tr_frag(L, o, o + 16 * 128);
        }
        constexpr int CC_ROW = 272, S_CC = 136192, S_CTX = S_CC + 32 * CC_ROW, S_WG = S_CTX + 512, S_RL = S_WG + 256;
        int ccw = S_CC + (8 * (g >> 1) + w) * CC_ROW + (8 * i + 4 * (g & 1)) * 2;
        int ccr = S_CC + ((w >> 2) * 16 + i) * CC_ROW + g * 16;
        int cxw = S_CTX + ((g >> 1) + 2 * (w >> 2)) * 128 + (32 * ((w >> 1) & 1) + 8 * (i >> 2) + 4 * (w & 1) + (i & 3)) * 2;
        int cxr = S_CTX + (i & 3) * 128 + g * 16;
        int po = g * 32768 + (i >> 3) * 2048 + (i & 7) * 16;
        const int ps = WS_OB + (8 * w + (h >> 4)) * 16384 + ch * 8192 + (h & 15) * 128;
        PIN(ccw); PIN(ccr); PIN(cxw); PIN(cxr); PIN(r5o); PIN(po);
        STAMP(4);
        if (w == 7) {
            v4u ml;
            for (unsigned it = 0;; ++it) {
                ml = ld_dev(rsW, i * 2048 + g * 512, WS_ST + (h >> 4) * 256 + (h & 15) * 16);
                if (__all(badtag(ml, tag) == 0u) || it > SPIN_CAP) break;
                __builtin_amdgcn_s_sleep(ST_SLEEP);
                asm volatile("" ::: "memory");
            }
            const float Mx = max_x32(max_x16(max8(bitsf(ml.x))));
            const float wgt = __builtin_amdgcn_exp2f(bitsf(ml.x) - Mx);
            *(LAS float*)(L + S_WG + lane * 4) = wgt;
            const float rl = __builtin_amdgcn_rcpf(sum_x32(sum_x16(sum8(wgt * bitsf(ml.z)))));
            if ((lane & 55) == 0) *(LAS float*)(L + S_RL + (lane >> 1)) = rl;
        }
        BARRIER();
        STAMP(2);
        v4u pr[2][2], a0, a1;
        float rL;
        {
            pr[0][0] = ld_dev(rsW, po, ps);
            pr[1][0] = ld_dev(rsW, po + 64 * 16384, ps);
            pr[0][1] = ld_dev(rsW, po, ps + 4096);
            pr[1][1] = ld_dev(rsW, po + 64 * 16384, ps + 4096);
            SCHED();
            const unsigned wg = *(LAS unsigned*)(L + S_WG + (16 * g + (i & 8) + w) * 4);
            rL = *(LAS float*)(L + S_RL + (g >> 1) * 4);
#pragma unroll
            for (int k = 0; k < 4; ++k) {
                a0[k] = __builtin_amdgcn_perm(wg, wg, i < 8 ? sel_of(i, k, 1) : 0x0c0c0c0cu);
                a1[k] = __builtin_amdgcn_perm(wg, wg, i < 8 ? 0x0c0c0c0cu : sel_of(i, k, 1));
            }
            PIN(a0); PIN(a1); PIN(rL);
            SCHED();
            for (unsigned it = 0;; ++it) {
                const unsigned mx = max(max(umax4(pr[0][0]), umax4(pr[1][0])), max(umax4(pr[0][1]), umax4(pr[1][1])));
                if (__all(mx != ~0u) || it > SPIN_CAP) break;
                asm volatile("" ::: "memory");
                pr[0][0] = ld_dev(rsW, po, ps);
                pr[1][0] = ld_dev(rsW, po + 64 * 16384, ps);
                pr[0][1] = ld_dev(rsW, po, ps + 4096);
                pr[1][1] = ld_dev(rsW, po + 64 * 16384, ps + 4096);
            }
        }
        STAMP(5);
        CSTAMP(12);
        {
            f32x4 d0 = {0.f, 0.f, 0.f, 0.f}, d1 = {0.f, 0.f, 0.f, 0.f};
            d0 = mfma(a0, pr[0][0], d0); d1 = mfma(a0, pr[0][1], d1);
            d0 = mfma(a1, pr[1][0], d0); d1 = mfma(a1, pr[1][1], d1);
            *(LAS v2u*)(L + ccw) = v2u{pk_bf16(d0.x, d0.y), pk_bf16(d0.z, d0.w)};
            *(LAS v2u*)(L + ccw + 16 * CC_ROW) = v2u{pk_bf16(d1.x, d1.y), pk_bf16(d1.z, d1.w)};
        }
        CSTAMP(13);
        BARRIER();
        CSTAMP(14);
        {
            v4u cf[4];
#pragma unroll
            for (int s = 0; s < 4; ++s) cf[s] = *(LAS v4u*)(L + ccr + s * 64);
            SCHED();
            f32x4 d = {0.f, 0.f, 0.f, 0.f};
#pragma unroll
            for (int s = 0; s < 4; ++s) d = mfma(cf[s], fuv[s], d);
            const float c = sum_x16((d.x + d.y) + (d.z + d.w)) * rL;
            if (!(g & 1)) *(LAS u16*)(L + cxw) = (u16)pk_bf16(c, c);
        }
        CSTAMP(15);
        BARRIER();
        CSTAMP(16);
        {
            const v4u c0 = *(LAS v4u*)(L + cxr), c1 = *(LAS v4u*)(L + cxr + 64);
            f32x4 d[4];
#pragma unroll
            for (int T = 0; T < 4; ++T) d[T] = mfma(fo[0][T], c0, f32x4{0.f, 0.f, 0.f, 0.f});
#pragma unroll
            for (int T = 0; T < 4; ++T) d[T] = mfma(fo[1][T], c1, d[T]);
            const f32x4 e0 = (i & 4) ? d[1] : d[0], e1 = (i & 4) ? d[3] : d[2];
            f32x4 e = (i & 8) ? e1 : e0;
            e.x += dpp<DPP_X2>(e.x); e.y += dpp<DPP_X2>(e.y); e.z += dpp<DPP_X2>(e.z); e.w += dpp<DPP_X2>(e.w);
            st_dev(v4u{fbits((i & 2) ? e.z : e.x), tag, fbits((i & 2) ? e.w : e.y), tag}, rsW, r5o, r5s);
        }
        STAMP(6);
        CSTAMP(18);
        FINISH();
        __builtin_amdgcn_s_waitcnt(0);
    }
}

extern "C" void kernel_launch(void* const* d_in, const int* in_sizes, int n_in, void* d_out, int out_size, void* d_ws, size_t ws_size,
                              hipStream_t stream) {
    fused_mla_ffn<<<256, 512, 0, stream>>>((const u16*)d_in[0], (const u16*)d_in[1], (const u16*)d_in[2], (const u16*)d_in[3], (const u16*)d_in[4],
                                           (const u16*)d_in[5], (const u16*)d_in[7], (const u16*)d_in[8], (const u16*)d_in[9], (const u16*)d_in[10],
                                           (u16*)d_out, (unsigned char*)d_ws);
}
```

```cpp
#define STAMPS
#define PROBE_K 0
#define PROBE_ROLE 0
#include <hip/hip_runtime.h>
#include <stdint.h>

typedef unsigned short u16;
typedef unsigned v4u __attribute__((ext_vector_type(4)));
typedef unsigned v2u __attribute__((ext_vector_type(2)));
typedef float f32x4 __attribute__((ext_vector_type(4)));
typedef float f32x2 __attribute__((ext_vector_type(2)));
typedef __bf16 bf16x8 __attribute__((ext_vector_type(8)));
typedef __bf16 bf16x2 __attribute__((ext_vector_type(2)));
typedef short s16x4 __attribute__((ext_vector_type(4)));
typedef unsigned short u16x2 __attribute__((ext_vector_type(2)));
#define LAS __attribute__((address_space(3)))
typedef __amdgpu_buffer_rsrc_t rsrc_t;

constexpr int AUX_NT = 2, AUX_SC1 = 16;
constexpr int NFFN = 86;
constexpr int WS_EPOCH = 0;
constexpr int WS_QG = 4096;
constexpr int WS_Q1 = WS_QG + 64 * 2048;
constexpr int WS_H1 = WS_Q1 + 256 * 2048;
constexpr int WS_OB = WS_H1 + 88 * 2048;
constexpr int WS_ST = WS_OB + 128 * 16384;
constexpr int WS_R5 = WS_ST + 128 * 256;
constexpr int R5_CONS = 216 * 128;
constexpr int WS_BYTES = WS_R5 + 64 * R5_CONS;
constexpr unsigned SPIN_CAP = 400000u;
constexpr int LDS_BYTES = 163840;
#define CODE_LINES 132
#define A_DELAY 24
#define B_DELAY 0
#define UK_DELAY 3
#define E1_DELAY 30
#define ST_SLEEP 2
#define E2_SLEEP 2
#define FIN_DELAY 60
#define POISON_AT 0
#define A_START 110
#define E2_START 440
#define FIN_START 270
#define LAG 2
#define UKPF 0
#define H1X 2

#define NSTAMP 20
#define CSTAMP(k) do { __builtin_amdgcn_sched_barrier(0); stamp[k] = __builtin_amdgcn_s_memtime(); __builtin_amdgcn_sched_barrier(0); } while (0)
#define STAMP(k) do { __builtin_amdgcn_sched_barrier(0); stamp[k] = __builtin_amdgcn_s_memrealtime(); __builtin_amdgcn_sched_barrier(0); } while (0)
#define FINISH() do { unsigned long long until = stamp[0] + 6000ull; \
        if (PROBE_K > 0 && (idx < 16 ? 0 : 1) == PROBE_ROLE && stamp[PROBE_K > 0 ? PROBE_K : 0] != 0ull) until += stamp[PROBE_K > 0 ? PROBE_K : 0] - stamp[0]; \
        while (__builtin_amdgcn_s_memrealtime() < until) __builtin_amdgcn_s_sleep(4); } while (0)

__device__ __forceinline__ f32x4 mfma(v4u a, v4u b, f32x4 c) {
    return __builtin_amdgcn_mfma_f32_16x16x32_bf16(__builtin_bit_cast(bf16x8, a), __builtin_bit_cast(bf16x8, b), c, 0, 0, 0);
}
__device__ __forceinline__ unsigned pk_bf16(float lo, float hi) {
    f32x2 p = {lo, hi};
    return __builtin_bit_cast(unsigned, __builtin_convertvector(p, bf16x2));
}
__device__ __forceinline__ float bf_lo(unsigned u) { return __builtin_bit_cast(float, u << 16); }
__device__ __forceinline__ float bf_hi(unsigned u) { return __builtin_bit_cast(float, u & 0xffff0000u); }
__device__ __forceinline__ float rbf(float x) { return bf_lo(pk_bf16(x, x)); }
__device__ __forceinline__ unsigned nosent(unsigned u) { return u < 0xfffffffeu ? u : 0xfffffffeu; }
__device__ __forceinline__ unsigned fbits(float f) { return __builtin_bit_cast(unsigned, f); }
__device__ __forceinline__ float bitsf(unsigned u) { return __builtin_bit_cast(float, u); }
__device__ __forceinline__ unsigned pk_sub_sat(unsigned a, unsigned b) {
    return __builtin_bit_cast(unsigned, __builtin_elementwise_sub_sat(__builtin_bit_cast(u16x2, a), __builtin_bit_cast(u16x2, b)));
}
__device__ __forceinline__ float vmax(float a, float b) { float r; asm("v_max_f32 %0, %1, %2" : "=v"(r) : "v"(a), "v"(b)); return r; }
__device__ __forceinline__ float vmax3(float a, float b, float c) { float r; asm("v_max3_f32 %0, %1, %2, %3" : "=v"(r) : "v"(a), "v"(b), "v"(c)); return r; }
template <int CTRL> __device__ __forceinline__ float dpp(float v) {
    return __builtin_bit_cast(float, __builtin_amdgcn_update_dpp(0, __builtin_bit_cast(int, v), CTRL, 0xf, 0xf, true));
}
constexpr int DPP_X1 = 0xB1, DPP_X2 = 0x4E, DPP_HMIR = 0x141, DPP_MIR = 0x140, DPP_ROR8 = 0x128;
__device__ __forceinline__ float sum_x16(float v) { auto r = __builtin_amdgcn_permlane16_swap(fbits(v), fbits(v), false, false); return bitsf(r[0]) + bitsf(r[1]); }
__device__ __forceinline__ float sum_x32(float v) { auto r = __builtin_amdgcn_permlane32_swap(fbits(v), fbits(v), false, false); return bitsf(r[0]) + bitsf(r[1]); }
__device__ __forceinline__ float max_x16(float v) { auto r = __builtin_amdgcn_permlane16_swap(fbits(v), fbits(v), false, false); return vmax(bitsf(r[0]), bitsf(r[1])); }
__device__ __forceinline__ float max_x32(float v) { auto r = __builtin_amdgcn_permlane32_swap(fbits(v), fbits(v), false, false); return vmax(bitsf(r[0]), bitsf(r[1])); }
__device__ __forceinline__ float sum8(float v) { v += dpp<DPP_X1>(v); v += dpp<DPP_X2>(v); v += dpp<DPP_HMIR>(v); return v; }
__device__ __forceinline__ float max8(float v) { v = vmax(v, dpp<DPP_X1>(v)); v = vmax(v, dpp<DPP_X2>(v)); v = vmax(v, dpp<DPP_HMIR>(v)); return v; }
__device__ __forceinline__ float sum64(float v) {
    v += dpp<DPP_X1>(v); v += dpp<DPP_X2>(v); v += dpp<DPP_HMIR>(v); v += dpp<DPP_MIR>(v);
    return sum_x32(sum_x16(v));
}

__device__ __forceinline__ rsrc_t mkrs(const void* p, int bytes) { return __builtin_amdgcn_make_buffer_rsrc((void*)p, 0, bytes, 0x00020000); }
__device__ __forceinline__ v4u ld_nt(rsrc_t r, int voff, int soff) { return __builtin_amdgcn_raw_buffer_load_b128(r, voff, soff, AUX_NT); }
__device__ __forceinline__ v4u ld_dev(rsrc_t r, int voff, int soff) { return __builtin_amdgcn_raw_buffer_load_b128(r, voff, soff, AUX_SC1); }
__device__ __forceinline__ void st_dev(v4u v, rsrc_t r, int voff, int soff) { __builtin_amdgcn_raw_buffer_store_b128(v, r, voff, soff, AUX_SC1); }
__device__ __forceinline__ void dma16(const void* sbase, unsigned voff, unsigned ldsdst) {
    asm volatile("s_mov_b32 m0, %2\n\ts_nop 0\n\tglobal_load_lds_dwordx4 %0, %1 nt" ::"v"(voff), "s"(sbase), "s"(ldsdst) : "memory");
}
#define VMCNT(n) asm volatile("s_waitcnt vmcnt(" #n ")" ::: "memory")
#define BARRIER() do { asm volatile("s_waitcnt lgkmcnt(0)" ::: "memory"); __builtin_amdgcn_s_barrier(); asm volatile("" ::: "memory"); } while (0)
#define PIN(x) asm volatile("" : "+v"(x))
#define PIN2() asm volatile("" ::: "memory")
#define SCHED() __builtin_amdgcn_sched_barrier(0)

__device__ __forceinline__ v4u tr_frag(LAS unsigned char* L, int off0, int off1) {
    s16x4 a = __builtin_amdgcn_ds_read_tr16_b64_v4i16((LAS s16x4*)(L + off0));
    s16x4 b = __builtin_amdgcn_ds_read_tr16_b64_v4i16((LAS s16x4*)(L + off1));
    v2u ua = __builtin_bit_cast(v2u, a), ub = __builtin_bit_cast(v2u, b);
    return v4u{ua.x, ua.y, ub.x, ub.y};
}

__device__ __forceinline__ void ln_stats(v4u xa, v4u xb, int i, float& mean, float& sd) {
    const unsigned f0 = __builtin_amdgcn_readlane(xa.x, 0), f1 = __builtin_amdgcn_readlane(xa.x, 8);
    const float p = bf_lo(i < 8 ? f0 : f1);
    float s0 = 0.f, s1 = 0.f, q0 = 0.f, q1 = 0.f;
#pragma unroll
    for (int e = 0; e < 4; ++e) {
        const float a0 = bf_lo(xa[e]) - p, a1 = bf_hi(xa[e]) - p, b0 = bf_lo(xb[e]) - p, b1 = bf_hi(xb[e]) - p;
        s0 += a0 + a1; s1 += b0 + b1;
        q0 = fmaf(a0, a0, fmaf(a1, a1, q0)); q1 = fmaf(b0, b0, fmaf(b1, b1, q1));
    }
    const float md = sum_x32(sum_x16(sum8(s0 + s1))) * (1.0f / 512.0f);
    const float var = fmaxf(fmaf(-md, md, sum_x32(sum_x16(sum8(q0 + q1))) * (1.0f / 512.0f)), 0.f);
    mean = rbf(md + p);
    sd = rbf(__builtin_amdgcn_sqrtf(rbf(rbf(var) + 1e-5f)));
}
__device__ __forceinline__ unsigned ln_apply(unsigned xv, float mean, float sd, float rc) {
    const unsigned d = pk_bf16(bf_lo(xv) - mean, bf_hi(xv) - mean);
    const float n0 = bf_lo(d), n1 = bf_hi(d);
    float t0 = n0 * rc, t1 = n1 * rc;
    t0 = fmaf(fmaf(-t0, sd, n0), rc, t0);
    t1 = fmaf(fmaf(-t1, sd, n1), rc, t1);
    return pk_bf16(t0, t1);
}
__device__ __forceinline__ unsigned sel_of(int i, int k, int odd) {
    const bool mine = ((i & 7) >> 1) == k;
    return mine ? ((i & 1) ? (odd ? 0x03020c0cu : 0x01000c0cu) : (odd ? 0x0c0c0302u : 0x0c0c0100u)) : 0x0c0c0c0cu;
}
__device__ __forceinline__ unsigned get_tag(unsigned epoch) { PIN(epoch); return epoch + 1u; }
__device__ __forceinline__ unsigned umax4(v4u v) { return max(max(v.x, v.y), max(v.z, v.w)); }
__device__ __forceinline__ unsigned badtag(v4u v, unsigned tag) { return (v.y ^ tag) | (v.w ^ tag); }

__global__ void __launch_bounds__(512, 2)
fused_mla_ffn(const u16* __restrict__ x, const u16* __restrict__ ckv, const u16* __restrict__ wq, const u16* __restrict__ wuk,
              const u16* __restrict__ wuv, const u16* __restrict__ wo, const u16* __restrict__ w1, const u16* __restrict__ b1,
              const u16* __restrict__ w2, const u16* __restrict__ b2, u16* __restrict__ out, unsigned char* ws) {
    __shared__ __attribute__((aligned(16))) unsigned char smem[LDS_BYTES];
    LAS unsigned char* L = (LAS unsigned char*)smem;
    unsigned long long stamp[NSTAMP] = {};
    STAMP(0);
    const unsigned long long tstart = __builtin_amdgcn_s_memrealtime();
    const int tid = threadIdx.x, lane = tid & 63, w = __builtin_amdgcn_readfirstlane(tid >> 6), i = lane & 15, g = lane >> 4;
    const int bid = blockIdx.x, xcd = bid & 7, idx = bid >> 3;
    unsigned codepf = 0;
    {
        unsigned long long pc;
        asm volatile("s_getpc_b64 %0" : "=s"(pc));
        const rsrc_t rsC = mkrs((const void*)(pc & ~127ull), CODE_LINES * 128);
        codepf = __builtin_amdgcn_raw_buffer_load_b32(rsC, (tid ^ 384) * 128, 0, 0);
    }
    SCHED();
    asm volatile("" ::"s"(x), "s"(ckv), "s"(wq), "s"(wuk), "s"(wuv), "s"(wo), "s"(w1), "s"(b1), "s"(w2), "s"(b2), "s"(out), "s"(ws));
    STAMP(11);
    const rsrc_t rsW = mkrs(ws, WS_BYTES);
    const rsrc_t rsX = mkrs(x, 2048);
    constexpr int S_UK = 98304;
    unsigned epoch = __builtin_amdgcn_raw_buffer_load_b32(rsW, WS_EPOCH, 0, AUX_SC1);
    PIN2();
    v4u xr0, xr1, xqa, xqb, a[16];
    unsigned ep1;
    if (idx < 16 && w < 2) {
        ep1 = __builtin_amdgcn_raw_buffer_load_b32(rsW, WS_EPOCH + 0 * lane, 0, AUX_SC1);
        PIN2();
        const int t8 = 2 * (idx & 3) + w, h1 = (4 * xcd + (idx >> 2)) ^ (2 + w);
        xr0 = __builtin_amdgcn_raw_buffer_load_b128(rsX, i * 128 + g * 32, 0, 0);
        xr1 = __builtin_amdgcn_raw_buffer_load_b128(rsX, i * 128 + g * 32 + 16, 0, 0);
        xqa = __builtin_amdgcn_raw_buffer_load_b128(rsX, (i >> 3) * 1024 + g * 32, t8 * 128, 0);
        xqb = __builtin_amdgcn_raw_buffer_load_b128(rsX, (i >> 3) * 1024 + g * 32 + 16, t8 * 128, 0);
        const rsrc_t rsQ = mkrs(wq, 512 * 8192);
#pragma unroll
        for (int u = 0; u < 16; ++u) a[u] = ld_nt(rsQ, g * 131072 + i * 16, (64 * t8 + u) * 8192 + h1 * 256);
        SCHED();
        STAMP(1);
    }
    if (idx < 16 && w >= 4) {
        if (UK_DELAY) __builtin_amdgcn_s_sleep(UK_DELAY);
        const unsigned char* base = (const unsigned char*)wuk + (size_t)((idx & 3) * 128 + 32 * (w - 4)) * 8192 + (4 * xcd + (idx >> 2)) * 256;
#pragma unroll
        for (int j = 0; j < 8; ++j) dma16(base + j * 32768, g * 8192 + ((i ^ ((4 * j + g) & 15)) * 16), S_UK + (w - 4) * 8192 + j * 1024);
        STAMP(1);
    }
    const v4u ones = {~0u, ~0u, ~0u, ~0u};
    const int q4 = (lane >> 2) & 3, p4 = lane & 3, trow = 4 * g + q4;

    if (__builtin_expect(idx >= 16, 0)) {
        const int hh = idx & 1, spa = xcd * 8 + ((idx - 16) >> 1), b = spa >> 5, aidx = spa * 2 + hh;
        const int tw = (w + 4 * hh) & 7;
        constexpr int QF = 1040, S_Q = 131072, S_P = S_Q + 16 * QF, S_M = S_P + 4096, S_L = S_M + 512;
        asm volatile("" ::"v"(codepf));
        PIN(epoch);
        while (__builtin_amdgcn_s_memrealtime() < tstart + A_START) __builtin_amdgcn_s_sleep(1);
        const int fidx = (aidx >> 1) + (aidx & 1) * 64;
        STAMP(1);
        {
            const unsigned char* base = (const unsigned char*)ckv + ((size_t)spa * 128 + 16 * tw) * 1024;
#pragma unroll
            for (int r = 0; r < 16; ++r) dma16(base + r * 1024, (lane ^ (((r & 7) << 1) | (r >> 3))) * 16, (16 * tw + r) * 1024);
        }
        st_dev(ones, rsW, tid * 16, WS_OB + aidx * 16384);
        st_dev(ones, rsW, tid * 16 + 8192, WS_OB + aidx * 16384);
        v4u kf[16], vf[4][4];
        VMCNT(2);
        const unsigned tag = get_tag(epoch);
        asm volatile("" ::"v"(codepf));
        STAMP(2);
        {
            const int X = ((i & 7) << 1) | (i >> 3);
#pragma unroll
            for (int s = 0; s < 16; ++s) kf[s] = *(LAS v4u*)(L + (16 * tw + i) * 1024 + g * 256 + ((s ^ X) * 16));
        }
        int qw = S_Q + ((lane & 31) >> 1) * QF + ((lane >> 5) * 16 + 2 * w) * 16 + (lane & 1) * 8;
        int qr = S_Q + lane * 16, pw = S_P + (tw >> 1) * 1024 + lane * 16 + (tw & 1) * 8, mw = S_M + i * 32 + tw * 4, mr = S_M + i * 32;
        int obo = (i & 7) * 128 + (i >> 3) * 64 + g * 16;
        PIN(qw); PIN(qr); PIN(pw); PIN(mw); PIN(mr); PIN(obo);
        BARRIER();
        STAMP(3);
        {
            v4u qg[4];
            const int qb = WS_QG + (b * 32 + hh * 16 + 2 * w) * 2048;
            const bool late = __builtin_amdgcn_s_memrealtime() >= tstart + E2_START;
            if (late) {
#pragma unroll
                for (int t = 0; t < 4; ++t) qg[t] = ld_dev(rsW, lane * 16 + t * 1024, qb);
            }
            SCHED();
            {
                const int X = ((trow & 7) << 1) | (trow >> 3);
#pragma unroll
                for (int T = 0; T < 4; ++T) {
                    const int o = trow * 1024 + (w >> 1) * 256 + (((8 * (w & 1) + 2 * T + (p4 >> 1)) ^ X) * 16) + 8 * (p4 & 1);
#pragma unroll
                    for (int s = 0; s < 4; ++s) vf[s][T] = tr_frag(L, o + s * 32768, o + s * 32768 + 16384);
                }
            }
            SCHED();
            if (!late) {
                while (__builtin_amdgcn_s_memrealtime() < tstart + E2_START) __builtin_amdgcn_s_sleep(1);
#pragma unroll
                for (int t = 0; t < 4; ++t) qg[t] = ld_dev(rsW, lane * 16 + t * 1024, qb);
            }
            for (unsigned it = 0;; ++it) {
                unsigned bad = 0u;
#pragma unroll
                for (int t = 0; t < 4; ++t) bad |= (qg[t].y ^ tag) | (qg[t].w ^ tag);
                if (__all(bad == 0u) || it > SPIN_CAP) break;
                asm volatile("" ::: "memory");
                __builtin_amdgcn_s_sleep(E2_SLEEP);
#pragma unroll
                for (int t = 0; t < 4; ++t)
                    if (__any(badtag(qg[t], tag) != 0u)) qg[t] = ld_dev(rsW, lane * 16 + t * 1024, qb);
            }
            VMCNT(0);
#pragma unroll
            for (int t = 0; t < 4; ++t) *(LAS v2u*)(L + qw + (t & 1) * 512 + (t >> 1) * 16) = v2u{qg[t].x, qg[t].z};
        }
        STAMP(4);
        CSTAMP(12);
        BARRIER();
        CSTAMP(13);
        float Mx;
        {
            f32x4 d = {0.f, 0.f, 0.f, 0.f};
            v4u qf[16];
#pragma unroll
            for (int s = 0; s < 16; ++s) qf[s] = *(LAS v4u*)(L + qr + s * QF);
            SCHED();
            if (fidx < NFFN) {
                const unsigned char* b2w = (const unsigned char*)w2 + (size_t)fidx * 16384 + w * 2048;
                dma16(b2w, lane * 16, w * 2048);
                dma16(b2w + 1024, lane * 16, w * 2048 + 1024);
            }
            SCHED();
#pragma unroll
            for (int s = 0; s < 16; ++s) d = mfma(kf[s], qf[s], d);
            const float SC = 0.08838834764831845f, LOG2E = 1.4426950408889634f;
            const unsigned r0 = pk_bf16(d.x, d.y), r1 = pk_bf16(d.z, d.w);
            const unsigned s0 = pk_bf16(bf_lo(r0) * SC, bf_hi(r0) * SC), s1 = pk_bf16(bf_lo(r1) * SC, bf_hi(r1) * SC);
            const float t0 = bf_lo(s0), t1 = bf_hi(s0), t2 = bf_lo(s1), t3 = bf_hi(s1);
            const float mt = max_x32(max_x16(vmax(vmax3(t0, t1, t2), t3)));
            if (g == 0) *(LAS float*)(L + mw) = mt;
            CSTAMP(14);
            BARRIER();
            const f32x4 ma = *(LAS f32x4*)(L + mr), mb = *(LAS f32x4*)(L + mr + 16);
            Mx = __builtin_ceilf(vmax3(vmax3(ma.x, ma.y, ma.z), vmax3(ma.w, mb.x, mb.y), vmax(mb.z, mb.w)) * LOG2E);
            const float p0 = __builtin_amdgcn_exp2f(fmaf(t0, LOG2E, -Mx)), p1 = __builtin_amdgcn_exp2f(fmaf(t1, LOG2E, -Mx));
            const float p2 = __builtin_amdgcn_exp2f(fmaf(t2, LOG2E, -Mx)), p3 = __builtin_amdgcn_exp2f(fmaf(t3, LOG2E, -Mx));
            *(LAS v2u*)(L + pw) = v2u{pk_bf16(p0, p1), pk_bf16(p2, p3)};
            *(LAS float*)(L + S_L + tw * 256 + lane * 4) = (p0 + p1) + (p2 + p3);
        }
        CSTAMP(15);
        BARRIER();
        STAMP(5);
        CSTAMP(16);
        {
            v4u pf[4];
#pragma unroll
            for (int s = 0; s < 4; ++s) pf[s] = *(LAS v4u*)(L + qr + (S_P - S_Q) + s * 1024);
            if (w == 4) {
                float ls = 0.f;
#pragma unroll
                for (int t = 0; t < 8; ++t) ls += *(LAS float*)(L + S_L + t * 256 + lane * 4);
                ls = sum_x32(sum_x16(ls));
                if (g == 0) st_dev(v4u{fbits(Mx), tag, fbits(ls), tag}, rsW, lane * 16, WS_ST + aidx * 256);
            }
            SCHED();
            f32x4 d[4];
#pragma unroll
            for (int T = 0; T < 4; ++T) d[T] = f32x4{0.f, 0.f, 0.f, 0.f};
#pragma unroll
            for (int s = 0; s < 4; ++s)
#pragma unroll
                for (int T = 0; T < 4; ++T) d[T] = mfma(vf[s][T], pf[s], d[T]);
            const v4u t0 = {nosent(pk_bf16(d[0].x, d[0].y)), nosent(pk_bf16(d[0].z, d[0].w)), nosent(pk_bf16(d[1].x, d[1].y)), nosent(pk_bf16(d[1].z, d[1].w))};
            const v4u t1 = {nosent(pk_bf16(d[2].x, d[2].y)), nosent(pk_bf16(d[2].z, d[2].w)), nosent(pk_bf16(d[3].x, d[3].y)), nosent(pk_bf16(d[3].z, d[3].w))};
            v4u sa, sb;
#pragma unroll
            for (int k = 0; k < 4; ++k) {
                sa[k] = (unsigned)__builtin_amdgcn_update_dpp((int)t0[k], (int)t1[k], DPP_ROR8, 0xf, 0xc, false);
                sb[k] = (unsigned)__builtin_amdgcn_update_dpp((int)t1[k], (int)t0[k], DPP_ROR8, 0xf, 0x3, false);
            }
            st_dev(sa, rsW, obo, WS_OB + aidx * 16384 + w * 2048);
            st_dev(sb, rsW, obo + 1024, WS_OB + aidx * 16384 + w * 2048);
        }
        STAMP(6);
        CSTAMP(18);
        const unsigned long long tpub = __builtin_amdgcn_s_memrealtime();
        if (fidx < NFFN) {
            constexpr int S_H = 16384;
            if (w == 0) {
                const unsigned bias = *(const unsigned*)(b1 + fidx * 16 + (lane & 1) * 8 + ((lane >> 1) & 1) * 4 + ((lane >> 3) & 1) * 2);
                const int ho = (lane >> 3) * 2048 + (((lane >> 1) & 3) * 16 + (lane & 1)) * 16, hs = WS_H1 + (fidx >> 3) * 16384 + (fidx & 7) * 32;
                v4u o0, o1;
                for (unsigned it = 0;; ++it) {
                    o0 = ld_dev(rsW, ho, hs);
                    o1 = ld_dev(rsW, ho + 1024, hs);
                    if (__all(((o0.y ^ tag) | (o0.w ^ tag) | (o1.y ^ tag) | (o1.w ^ tag)) == 0u) || it > SPIN_CAP) break;
                    asm volatile("" ::: "memory");
                }
                float v[4] = {bitsf(o0.x), bitsf(o0.z), bitsf(o1.x), bitsf(o1.z)};
#pragma unroll
                for (int e = 0; e < 4; ++e) { v[e] += dpp<DPP_ROR8>(v[e]); v[e] = sum_x32(sum_x16(v[e])); }
                float hq[2];
                const float va = (lane & 8) ? v[2] : v[0], vb = (lane & 8) ? v[3] : v[1];
#pragma unroll
                for (int e = 0; e < 2; ++e) {
                    const float hv = rbf(rbf(e ? vb : va) + (e ? bf_hi(bias) : bf_lo(bias)));
                    const float u2 = 2.0f * 0.7978845608028654f * 1.4426950408889634f * (hv + 0.044715f * hv * hv * hv);
                    const float th = 1.0f - 2.0f / (__builtin_amdgcn_exp2f(u2) + 1.0f);
                    hq[e] = 0.5f * hv * (1.0f + th);
                }
                if (lane < 16) *(LAS unsigned*)(L + S_H + ((lane >> 2) & 1) * 32 + ((lane & 1) * 8 + ((lane >> 1) & 1) * 4 + ((lane >> 3) & 1) * 2) * 2) = pk_bf16(hq[0], hq[1]);
            }
            VMCNT(2);
            BARRIER();
            if (w < 4) {
                const v2u hp = *(LAS v2u*)(L + S_H + (i >> 3) * 32 + g * 8);
                f32x4 y = {0.f, 0.f, 0.f, 0.f};
#pragma unroll
                for (int u = 0; u < 4; ++u) {
                    v4u af;
#pragma unroll
                    for (int k = 0; k < 4; ++k) af[k] = __builtin_amdgcn_perm(hp[u >> 1], hp[u >> 1], sel_of(i, k, u & 1));
                    y = mfma(af, *(LAS v4u*)(L + (4 * g + u) * 1024 + w * 256 + i * 16), y);
                }
                const int yo = ((g >> 1) * 32 + (i >> 1)) * R5_CONS + ((i & 1) * 4 + (g & 1) * 2) * 16, ys = WS_R5 + 8 * w * R5_CONS + (128 + fidx) * 128;
                st_dev(v4u{fbits(y.x), tag, fbits(y.y), tag}, rsW, yo, ys);
                st_dev(v4u{fbits(y.z), tag, fbits(y.w), tag}, rsW, yo + 16, ys);
            }
            STAMP(7);
        }
        if (aidx & 1) {
            const int c64 = aidx >> 1, rb = WS_R5 + c64 * R5_CONS;
            constexpr int S_RY = 32768, S_RS = S_RY + 512;
            const unsigned xo = x[c64 * 16 + i], bo = b2[(c64 & 31) * 16 + i];
            while (__builtin_amdgcn_s_memrealtime() < tpub + FIN_START) __builtin_amdgcn_s_sleep(2);
            v4u r0, r1, y0, y1;
            const bool has2 = 64 + 8 * w + (lane >> 3) < NFFN;
            STAMP(8);
            y0 = ld_dev(rsW, lane * 16, rb + 16384 + w * 1024);
            y1 = ld_dev(rsW, (has2 ? 8192 : 0) + lane * 16, rb + 16384 + w * 1024);
            r0 = ld_dev(rsW, lane * 16, rb + w * 2048);
            r1 = ld_dev(rsW, lane * 16 + 1024, rb + w * 2048);
            for (unsigned it = 0;; ++it) {
                if (__all((badtag(r0, tag) | badtag(r1, tag) | badtag(y0, tag) | badtag(y1, tag)) == 0u) || it > SPIN_CAP) break;
                asm volatile("" ::: "memory");
                if (__any((badtag(y0, tag) | badtag(y1, tag)) != 0u)) {
                    y0 = ld_dev(rsW, lane * 16, rb + 16384 + w * 1024);
                    y1 = ld_dev(rsW, (has2 ? 8192 : 0) + lane * 16, rb + 16384 + w * 1024);
                }
                if (__any(badtag(r0, tag) != 0u)) r0 = ld_dev(rsW, lane * 16, rb + w * 2048);
                if (__any(badtag(r1, tag) != 0u)) r1 = ld_dev(rsW, lane * 16 + 1024, rb + w * 2048);
            }
            STAMP(9);
            {
                float v[4] = {bitsf(r0.x) + bitsf(r1.x), bitsf(r0.z) + bitsf(r1.z), bitsf(y0.x) + (has2 ? bitsf(y1.x) : 0.f), bitsf(y0.z) + (has2 ? bitsf(y1.z) : 0.f)};
#pragma unroll
                for (int e = 0; e < 4; ++e) { v[e] += dpp<DPP_ROR8>(v[e]); v[e] = sum_x32(sum_x16(v[e])); }
                if (lane < 8) { *(LAS f32x2*)(L + S_RS + w * 64 + lane * 8) = f32x2{v[0], v[1]}; *(LAS f32x2*)(L + S_RY + w * 64 + lane * 8) = f32x2{v[2], v[3]}; }
            }
            BARRIER();
            if (w == 0) {
                const float s = sum_x32(sum_x16(*(LAS float*)(L + S_RS + g * 128 + i * 4) + *(LAS float*)(L + S_RS + g * 128 + 64 + i * 4)));
                const float ys = sum_x32(sum_x16(*(LAS float*)(L + S_RY + g * 128 + i * 4) + *(LAS float*)(L + S_RY + g * 128 + 64 + i * 4)));
                const float y = rbf(rbf(ys) + bf_lo(bo));
                const float gt = rbf(y * __builtin_amdgcn_rcpf(1.0f + __builtin_amdgcn_exp2f(-1.4426950408889634f * y)));
                if (g == 0) out[c64 * 16 + i] = (u16)pk_bf16(bf_lo(xo) + rbf(rbf(s) * gt), 0.f);
                if (c64 == 0 && lane == 0) __builtin_amdgcn_raw_buffer_store_b32(tag, rsW, WS_EPOCH, 0, AUX_SC1);
            }
            STAMP(10);
        }
        FINISH();
        return;
    }
    {
        const int h = 4 * xcd + (idx >> 2), cq = idx & 3, fb = h * 4 + cq;
        constexpr int S_WO = 0, S_UV = 65536, S_QB = 131072;
        v4u uk[2][4], af[16];
        const int t8 = 2 * cq + (w & 1);
        if (__builtin_expect(w < 2, 1)) {
            const int h1 = h ^ (2 + w);
            unsigned sl[2][4];
#pragma unroll
            for (int k = 0; k < 4; ++k) { sl[0][k] = sel_of(i, k, 0); sl[1][k] = sel_of(i, k, 1); PIN(sl[0][k]); PIN(sl[1][k]); }
            SCHED();
            float mean, sd;
            ln_stats(xr0, xr1, i, mean, sd);
            const float rc = __builtin_amdgcn_rcpf(sd);
            const unsigned tg = get_tag(ep1);
            STAMP(7);
            f32x4 acc = {0.f, 0.f, 0.f, 0.f};
#pragma unroll
            for (int e = 0; e < 8; ++e) {
                const unsigned xp = ln_apply(e < 4 ? xqa[e & 3] : xqb[e & 3], mean, sd, rc);
#pragma unroll
                for (int k = 0; k < 4; ++k) { af[2 * e][k] = __builtin_amdgcn_perm(xp, xp, sl[0][k]); af[2 * e + 1][k] = __builtin_amdgcn_perm(xp, xp, sl[1][k]); }
                if (e >= LAG) { acc = mfma(af[2 * (e - LAG)], a[2 * (e - LAG)], acc); acc = mfma(af[2 * (e - LAG) + 1], a[2 * (e - LAG) + 1], acc); }
                SCHED();
            }
#pragma unroll
            for (int u = 16 - 2 * LAG; u < 16; ++u) acc = mfma(af[u], a[u], acc);
            st_dev(v4u{fbits(acc.x), tg, fbits(acc.y), tg}, rsW, lane * 16, WS_Q1 + (h1 * 8 + t8) * 2048);
            st_dev(v4u{fbits(acc.z), tg, fbits(acc.w), tg}, rsW, lane * 16 + 1024, WS_Q1 + (h1 * 8 + t8) * 2048);
            STAMP(19);
            int qo = S_QB + (g >> 1) * 256 + (8 * i + 4 * (g & 1) + 2 * w) * 2;
            PIN(qo);
            if (E1_DELAY) __builtin_amdgcn_s_sleep(E1_DELAY); else VMCNT(0);
            STAMP(17);
            v4u o[8];
#pragma unroll
            for (int k = 0; k < 8; ++k) o[k] = ld_dev(rsW, lane * 16 + (k & 1) * 2048, WS_Q1 + h * 16384 + w * 1024 + (k >> 1) * 4096);
            for (unsigned it = 0;; ++it) {
                unsigned bad = 0u;
#pragma unroll
                for (int k = 0; k < 8; ++k) bad |= badtag(o[k], tg);
                if (__all(bad == 0u) || it > SPIN_CAP) break;
                asm volatile("" ::: "memory");
#pragma unroll
                for (int k = 0; k < 8; ++k)
                    if (__any(badtag(o[k], tg) != 0u)) o[k] = ld_dev(rsW, lane * 16 + (k & 1) * 2048, WS_Q1 + h * 16384 + w * 1024 + (k >> 1) * 4096);
            }
            STAMP(9);
            *(LAS unsigned*)(L + qo) = pk_bf16(((bitsf(o[0].x) + bitsf(o[1].x)) + (bitsf(o[2].x) + bitsf(o[3].x))) + ((bitsf(o[4].x) + bitsf(o[5].x)) + (bitsf(o[6].x) + bitsf(o[7].x))),
                                               ((bitsf(o[0].z) + bitsf(o[1].z)) + (bitsf(o[2].z) + bitsf(o[3].z))) + ((bitsf(o[4].z) + bitsf(o[5].z)) + (bitsf(o[6].z) + bitsf(o[7].z))));
        } else if (w >= 4) {
            VMCNT(0);
#pragma unroll
            for (int t = 0; t < 2; ++t)
#pragma unroll
                for (int s = 0; s < 4; ++s) uk[t][s] = *(LAS v4u*)(L + S_UK + (w - 4) * 8192 + (16 * t + i) * 256 + (((4 * s + g) ^ i) * 16));
            STAMP(8);
        }
        const unsigned tag = get_tag(epoch);
        int qro = S_QB + (i & 1) * 256 + g * 16, pubo = (i & 1) * 65536 + ((i >> 1) * 4 + g) * 16;
        PIN(qro); PIN(pubo);
        BARRIER();
        if (w >= 4) {
            v4u qf[4];
#pragma unroll
            for (int s = 0; s < 4; ++s) qf[s] = *(LAS v4u*)(L + qro + s * 64);
            SCHED();
            f32x4 d0 = {0.f, 0.f, 0.f, 0.f}, d1 = {0.f, 0.f, 0.f, 0.f};
#pragma unroll
            for (int s = 0; s < 4; ++s) { d0 = mfma(uk[0][s], qf[s], d0); d1 = mfma(uk[1][s], qf[s], d1); }
            const f32x4 d = (i & 2) ? d1 : d0;
            if (i < 4)
                st_dev(v4u{pk_bf16(d.x, d.y), tag, pk_bf16(d.z, d.w), tag}, rsW, pubo, WS_QG + h * 2048 + cq * 512 + (w - 4) * 128);
        } else {
            __builtin_amdgcn_s_sleep(7);
        }
        STAMP(3);
        asm volatile("" ::"v"(codepf));
        const int ch = cq >> 1, dh = cq & 1, slab = (w + 4 * ch) & 7;
        int r5o = ((i & 1) * 32 + (i >> 2)) * R5_CONS + (2 * g + ((i >> 1) & 1)) * 16;
        const int r5s = WS_R5 + 4 * slab * R5_CONS + fb * 128;
        if (B_DELAY) __builtin_amdgcn_s_sleep(B_DELAY);
        const int nh = (h >> 2) * 3 + ((h & 3) ? (h & 3) - 1 : 0), cb = nh >> 1;
        const bool ffn1 = (h & 3) != 1 && cb < 11 && w == (nh & 1);
        if (ffn1) {
            const unsigned char* base = (const unsigned char*)w1 + (size_t)(64 * t8) * 2752 + cb * 256;
            const unsigned vo = g * (16 * 2752) + (cb * 256 + i * 16 < 2752 ? i * 16 : 0);
#pragma unroll
            for (int u = 0; u < 16; ++u) dma16(base + u * 2752, vo, S_UK + u * 1024);
        }
        {
            const int r8 = lane >> 3;
            const unsigned sw = ((lane & 7) ^ (((r8 >> 1) & 3) << 1)) * 16;
            const unsigned char* base = (const unsigned char*)wuv + (size_t)(ch * 256 + 32 * w) * 8192 + h * 256 + dh * 128;
#pragma unroll
            for (int j = 0; j < 4; ++j) dma16(base + j * 65536, r8 * 8192 + sw, S_UV + (32 * w + 8 * j) * 128);
            const unsigned char* bo = (const unsigned char*)wo + (size_t)h * 131072 + dh * 65536 + slab * 128;
#pragma unroll
            for (int j = 0; j < 8; ++j) dma16(bo + j * 8192, r8 * 1024 + sw, S_WO + w * 8192 + j * 1024);
        }
        if (ffn1) {
            VMCNT(12);
            f32x4 acc = {0.f, 0.f, 0.f, 0.f};
#pragma unroll
            for (int u = 0; u < 16; ++u) acc = mfma(af[u], *(LAS v4u*)(L + S_UK + u * 1024 + lane * 16), acc);
            st_dev(v4u{fbits(acc.x), tag, fbits(acc.y), tag}, rsW, lane * 16, WS_H1 + (cb * 8 + t8) * 2048);
            st_dev(v4u{fbits(acc.z), tag, fbits(acc.w), tag}, rsW, lane * 16 + 1024, WS_H1 + (cb * 8 + t8) * 2048);
            STAMP(10);
            VMCNT(2);
        } else {
            VMCNT(0);
        }
        v4u fuv[4], fo[2][4];
        const int tsw = ((trow >> 1) & 3) << 1;
#pragma unroll
        for (int s = 0; s < 2; ++s)
#pragma unroll
            for (int T = 0; T < 4; ++T) {
                const int o = S_WO + w * 8192 + (s * 32 + trow) * 128 + (((2 * T + (p4 >> 1)) ^ tsw) * 16) + 8 * (p4 & 1);
                fo[s][T] = tr_frag(L, o, o + 16 * 128);
            }
        BARRIER();
#pragma unroll
        for (int s = 0; s < 4; ++s) {
            const int o = S_UV + (128 * (w >> 2) + 32 * s + trow) * 128 + (((2 * (w & 3) + (p4 >> 1)) ^ tsw) * 16) + 8 * (p4 & 1);
            fuv[s] = tr_frag(L, o, o + 16 * 128);
        }
        constexpr int CC_ROW = 272, S_CC = 136192, S_CTX = S_CC + 32 * CC_ROW, S_WG = S_CTX + 512, S_RL = S_WG + 256;
        int ccw = S_CC + (8 * (g >> 1) + w) * CC_ROW + (8 * i + 4 * (g & 1)) * 2;
        int ccr = S_CC + ((w >> 2) * 16 + i) * CC_ROW + g * 16;
        int cxw = S_CTX + ((g >> 1) + 2 * (w >> 2)) * 128 + (32 * ((w >> 1) & 1) + 8 * (i >> 2) + 4 * (w & 1) + (i & 3)) * 2;
        int cxr = S_CTX + (i & 3) * 128 + g * 16;
        int po = g * 32768 + (i >> 3) * 2048 + (i & 7) * 16;
        const int ps = WS_OB + (8 * w + (h >> 4)) * 16384 + ch * 8192 + (h & 15) * 128;
        PIN(ccw); PIN(ccr); PIN(cxw); PIN(cxr); PIN(r5o); PIN(po);
        STAMP(4);
        if (w == 7) {
            v4u ml;
            for (unsigned it = 0;; ++it) {
                ml = ld_dev(rsW, i * 2048 + g * 512, WS_ST + (h >> 4) * 256 + (h & 15) * 16);
                if (__all(badtag(ml, tag) == 0u) || it > SPIN_CAP) break;
                __builtin_amdgcn_s_sleep(ST_SLEEP);
                asm volatile("" ::: "memory");
            }
            const float Mx = max_x32(max_x16(max8(bitsf(ml.x))));
            const float wgt = __builtin_amdgcn_exp2f(bitsf(ml.x) - Mx);
            *(LAS float*)(L + S_WG + lane * 4) = wgt;
            const float rl = __builtin_amdgcn_rcpf(sum_x32(sum_x16(sum8(wgt * bitsf(ml.z)))));
            if ((lane & 55) == 0) *(LAS float*)(L + S_RL + (lane >> 1)) = rl;
        }
        BARRIER();
        STAMP(2);
        v4u pr[2][2], a0, a1;
        float rL;
        {
            pr[0][0] = ld_dev(rsW, po, ps);
            pr[1][0] = ld_dev(rsW, po + 64 * 16384, ps);
            pr[0][1] = ld_dev(rsW, po, ps + 4096);
            pr[1][1] = ld_dev(rsW, po + 64 * 16384, ps + 4096);
            SCHED();
            const unsigned wg = *(LAS unsigned*)(L + S_WG + (16 * g + (i & 8) + w) * 4);
            rL = *(LAS float*)(L + S_RL + (g >> 1) * 4);
#pragma unroll
            for (int k = 0; k < 4; ++k) {
                a0[k] = __builtin_amdgcn_perm(wg, wg, i < 8 ? sel_of(i, k, 1) : 0x0c0c0c0cu);
                a1[k] = __builtin_amdgcn_perm(wg, wg, i < 8 ? 0x0c0c0c0cu : sel_of(i, k, 1));
            }
            PIN(a0); PIN(a1); PIN(rL);
            SCHED();
            for (unsigned it = 0;; ++it) {
                const unsigned mx = max(max(umax4(pr[0][0]), umax4(pr[1][0])), max(umax4(pr[0][1]), umax4(pr[1][1])));
                if (__all(mx != ~0u) || it > SPIN_CAP) break;
                asm volatile("" ::: "memory");
                pr[0][0] = ld_dev(rsW, po, ps);
                pr[1][0] = ld_dev(rsW, po + 64 * 16384, ps);
                pr[0][1] = ld_dev(rsW, po, ps + 4096);
                pr[1][1] = ld_dev(rsW, po + 64 * 16384, ps + 4096);
            }
        }
        STAMP(5);
        CSTAMP(12);
        {
            f32x4 d0 = {0.f, 0.f, 0.f, 0.f}, d1 = {0.f, 0.f, 0.f, 0.f};
            d0 = mfma(a0, pr[0][0], d0); d1 = mfma(a0, pr[0][1], d1);
            d0 = mfma(a1, pr[1][0], d0); d1 = mfma(a1, pr[1][1], d1);
            *(LAS v2u*)(L + ccw) = v2u{pk_bf16(d0.x, d0.y), pk_bf16(d0.z, d0.w)};
            *(LAS v2u*)(L + ccw + 16 * CC_ROW) = v2u{pk_bf16(d1.x, d1.y), pk_bf16(d1.z, d1.w)};
        }
        CSTAMP(13);
        BARRIER();
        CSTAMP(14);
        {
            v4u cf[4];
#pragma unroll
            for (int s = 0; s < 4; ++s) cf[s] = *(LAS v4u*)(L + ccr + s * 64);
            SCHED();
            f32x4 d = {0.f, 0.f, 0.f, 0.f};
#pragma unroll
            for (int s = 0; s < 4; ++s) d = mfma(cf[s], fuv[s], d);
            const float c = sum_x16((d.x + d.y) + (d.z + d.w)) * rL;
            if (!(g & 1)) *(LAS u16*)(L + cxw) = (u16)pk_bf16(c, c);
        }
        CSTAMP(15);
        BARRIER();
        CSTAMP(16);
        {
            const v4u c0 = *(LAS v4u*)(L + cxr), c1 = *(LAS v4u*)(L + cxr + 64);
            f32x4 d[4];
#pragma unroll
            for (int T = 0; T < 4; ++T) d[T] = mfma(fo[0][T], c0, f32x4{0.f, 0.f, 0.f, 0.f});
#pragma unroll
            for (int T = 0; T < 4; ++T) d[T] = mfma(fo[1][T], c1, d[T]);
            const f32x4 e0 = (i & 4) ? d[1] : d[0], e1 = (i & 4) ? d[3] : d[2];
            f32x4 e = (i & 8) ? e1 : e0;
            e.x += dpp<DPP_X2>(e.x); e.y += dpp<DPP_X2>(e.y); e.z += dpp<DPP_X2>(e.z); e.w += dpp<DPP_X2>(e.w);
            st_dev(v4u{fbits((i & 2) ? e.z : e.x), tag, fbits((i & 2) ? e.w : e.y), tag}, rsW, r5o, r5s);
        }
        STAMP(6);
        CSTAMP(18);
        FINISH();
        __builtin_amdgcn_s_waitcnt(0);
    }
}

extern "C" void kernel_launch(void* const* d_in, const int* in_sizes, int n_in, void* d_out, int out_size, void* d_ws, size_t ws_size,
                              hipStream_t stream) {
    fused_mla_ffn<<<256, 512, 0, stream>>>((const u16*)d_in[0], (const u16*)d_in[1], (const u16*)d_in[2], (const u16*)d_in[3], (const u16*)d_in[4],
                                           (const u16*)d_in[5], (const u16*)d_in[7], (const u16*)d_in[8], (const u16*)d_in[9], (const u16*)d_in[10],
                                           (u16*)d_out, (unsigned char*)d_ws);
}
```

```cpp
#include <hip/hip_runtime.h>
#include <stdint.h>

typedef unsigned short u16;
typedef unsigned v4u __attribute__((ext_vector_type(4)));
typedef unsigned v2u __attribute__((ext_vector_type(2)));
typedef float f32x4 __attribute__((ext_vector_type(4)));
typedef float f32x2 __attribute__((ext_vector_type(2)));
typedef __bf16 bf16x8 __attribute__((ext_vector_type(8)));
typedef __bf16 bf16x2 __attribute__((ext_vector_type(2)));
typedef short s16x4 __attribute__((ext_vector_type(4)));
typedef unsigned short u16x2 __attribute__((ext_vector_type(2)));
#define LAS __attribute__((address_space(3)))
typedef __amdgpu_buffer_rsrc_t rsrc_t;

constexpr int AUX_NT = 2, AUX_SC1 = 16;
constexpr int NFFN = 86;
constexpr int WS_EPOCH = 0;
constexpr int WS_QG = 4096;
constexpr int WS_Q1 = WS_QG + 64 * 2048;
constexpr int WS_H1 = WS_Q1 + 256 * 2048;
constexpr int WS_OB = WS_H1 + 88 * 2048;
constexpr int WS_ST = WS_OB + 128 * 16384;
constexpr int WS_R5 = WS_ST + 128 * 256;
constexpr int R5_CONS = 216 * 128;
constexpr int WS_BYTES = WS_R5 + 64 * R5_CONS;
constexpr unsigned SPIN_CAP = 400000u;
constexpr int LDS_BYTES = 163840;
#define CODE_LINES 140
#define A_DELAY 24
#define B_DELAY 0
#define UK_DELAY 3
#define E1_DELAY 30
#define ST_SLEEP 2
#define E2_SLEEP 2
#define FIN_DELAY 60
#define POISON_AT 0
#define A_START 110
#define E2_START 420
#define FIN_START 270
#define LAG 2
#define UKPF 0
#define WS_BASE 0
#define FINY_START 200
#define WQ_FIRST 0
#define W2_DELAY 6
#define H1X 2

#define STAMP(k) do { } while (0)
#define CSTAMP(k) do { } while (0)
#define FINISH() do { } while (0)

__device__ __forceinline__ f32x4 mfma(v4u a, v4u b, f32x4 c) {
    return __builtin_amdgcn_mfma_f32_16x16x32_bf16(__builtin_bit_cast(bf16x8, a), __builtin_bit_cast(bf16x8, b), c, 0, 0, 0);
}
__device__ __forceinline__ unsigned pk_bf16(float lo, float hi) {
    f32x2 p = {lo, hi};
    return __builtin_bit_cast(unsigned, __builtin_convertvector(p, bf16x2));
}
__device__ __forceinline__ float bf_lo(unsigned u) { return __builtin_bit_cast(float, u << 16); }
__device__ __forceinline__ float bf_hi(unsigned u) { return __builtin_bit_cast(float, u & 0xffff0000u); }
__device__ __forceinline__ float rbf(float x) { return bf_lo(pk_bf16(x, x)); }
__device__ __forceinline__ unsigned nosent(unsigned u) { return u < 0xfffffffeu ? u : 0xfffffffeu; }
__device__ __forceinline__ unsigned fbits(float f) { return __builtin_bit_cast(unsigned, f); }
__device__ __forceinline__ float bitsf(unsigned u) { return __builtin_bit_cast(float, u); }
__device__ __forceinline__ unsigned pk_sub_sat(unsigned a, unsigned b) {
    return __builtin_bit_cast(unsigned, __builtin_elementwise_sub_sat(__builtin_bit_cast(u16x2, a), __builtin_bit_cast(u16x2, b)));
}
__device__ __forceinline__ float vmax(float a, float b) { float r; asm("v_max_f32 %0, %1, %2" : "=v"(r) : "v"(a), "v"(b)); return r; }
__device__ __forceinline__ float vmax3(float a, float b, float c) { float r; asm("v_max3_f32 %0, %1, %2, %3" : "=v"(r) : "v"(a), "v"(b), "v"(c)); return r; }
template <int CTRL> __device__ __forceinline__ float dpp(float v) {
    return __builtin_bit_cast(float, __builtin_amdgcn_update_dpp(0, __builtin_bit_cast(int, v), CTRL, 0xf, 0xf, true));
}
constexpr int DPP_X1 = 0xB1, DPP_X2 = 0x4E, DPP_HMIR = 0x141, DPP_MIR = 0x140, DPP_ROR8 = 0x128;
__device__ __forceinline__ float sum_x16(float v) { auto r = __builtin_amdgcn_permlane16_swap(fbits(v), fbits(v), false, false); return bitsf(r[0]) + bitsf(r[1]); }
__device__ __forceinline__ float sum_x32(float v) { auto r = __builtin_amdgcn_permlane32_swap(fbits(v), fbits(v), false, false); return bitsf(r[0]) + bitsf(r[1]); }
__device__ __forceinline__ float max_x16(float v) { auto r = __builtin_amdgcn_permlane16_swap(fbits(v), fbits(v), false, false); return vmax(bitsf(r[0]), bitsf(r[1])); }
__device__ __forceinline__ float max_x32(float v) { auto r = __builtin_amdgcn_permlane32_swap(fbits(v), fbits(v), false, false); return vmax(bitsf(r[0]), bitsf(r[1])); }
__device__ __forceinline__ float sum8(float v) { v += dpp<DPP_X1>(v); v += dpp<DPP_X2>(v); v += dpp<DPP_HMIR>(v); return v; }
__device__ __forceinline__ float max8(float v) { v = vmax(v, dpp<DPP_X1>(v)); v = vmax(v, dpp<DPP_X2>(v)); v = vmax(v, dpp<DPP_HMIR>(v)); return v; }
__device__ __forceinline__ float sum64(float v) {
    v += dpp<DPP_X1>(v); v += dpp<DPP_X2>(v); v += dpp<DPP_HMIR>(v); v += dpp<DPP_MIR>(v);
    return sum_x32(sum_x16(v));
}

__device__ __forceinline__ rsrc_t mkrs(const void* p, int bytes) { return __builtin_amdgcn_make_buffer_rsrc((void*)p, 0, bytes, 0x00020000); }
__device__ __forceinline__ v4u ld_nt(rsrc_t r, int voff, int soff) { return __builtin_amdgcn_raw_buffer_load_b128(r, voff, soff, AUX_NT); }
__device__ __forceinline__ v4u ld_dev(rsrc_t r, int voff, int soff) { return __builtin_amdgcn_raw_buffer_load_b128(r, voff, soff, AUX_SC1); }
__device__ __forceinline__ void st_dev(v4u v, rsrc_t r, int voff, int soff) { __builtin_amdgcn_raw_buffer_store_b128(v, r, voff, soff, AUX_SC1); }
__device__ __forceinline__ void dma16(const void* sbase, unsigned voff, unsigned ldsdst) {
    asm volatile("s_mov_b32 m0, %2\n\ts_nop 0\n\tglobal_load_lds_dwordx4 %0, %1 nt" ::"v"(voff), "s"(sbase), "s"(ldsdst) : "memory");
}
#define VMCNT(n) asm volatile("s_waitcnt vmcnt(" #n ")" ::: "memory")
#define BARRIER() do { asm volatile("s_waitcnt lgkmcnt(0)" ::: "memory"); __builtin_amdgcn_s_barrier(); asm volatile("" ::: "memory"); } while (0)
#define PIN(x) asm volatile("" : "+v"(x))
#define PIN2() asm volatile("" ::: "memory")
#define SCHED() __builtin_amdgcn_sched_barrier(0)

__device__ __forceinline__ v4u tr_frag(LAS unsigned char* L, int off0, int off1) {
    s16x4 a = __builtin_amdgcn_ds_read_tr16_b64_v4i16((LAS s16x4*)(L + off0));
    s16x4 b = __builtin_amdgcn_ds_read_tr16_b64_v4i16((LAS s16x4*)(L + off1));
    v2u ua = __builtin_bit_cast(v2u, a), ub = __builtin_bit_cast(v2u, b);
    return v4u{ua.x, ua.y, ub.x, ub.y};
}

__device__ __forceinline__ void ln_stats(v4u xa, v4u xb, int i, float& mean, float& sd) {
    const unsigned f0 = __builtin_amdgcn_readlane(xa.x, 0), f1 = __builtin_amdgcn_readlane(xa.x, 8);
    const float p = bf_lo(i < 8 ? f0 : f1);
    float s0 = 0.f, s1 = 0.f, q0 = 0.f, q1 = 0.f;
#pragma unroll
    for (int e = 0; e < 4; ++e) {
        const float a0 = bf_lo(xa[e]) - p, a1 = bf_hi(xa[e]) - p, b0 = bf_lo(xb[e]) - p, b1 = bf_hi(xb[e]) - p;
        s0 += a0 + a1; s1 += b0 + b1;
        q0 = fmaf(a0, a0, fmaf(a1, a1, q0)); q1 = fmaf(b0, b0, fmaf(b1, b1, q1));
    }
    const float md = sum_x32(sum_x16(sum8(s0 + s1))) * (1.0f / 512.0f);
    const float var = fmaxf(fmaf(-md, md, sum_x32(sum_x16(sum8(q0 + q1))) * (1.0f / 512.0f)), 0.f);
    mean = rbf(md + p);
    sd = rbf(__builtin_amdgcn_sqrtf(rbf(rbf(var) + 1e-5f)));
}
__device__ __forceinline__ unsigned ln_apply(unsigned xv, float mean, float sd, float rc) {
    const unsigned d = pk_bf16(bf_lo(xv) - mean, bf_hi(xv) - mean);
    const float n0 = bf_lo(d), n1 = bf_hi(d);
    float t0 = n0 * rc, t1 = n1 * rc;
    t0 = fmaf(fmaf(-t0, sd, n0), rc, t0);
    t1 = fmaf(fmaf(-t1, sd, n1), rc, t1);
    return pk_bf16(t0, t1);
}
__device__ __forceinline__ unsigned sel_of(int i, int k, int odd) {
    const bool mine = ((i & 7) >> 1) == k;
    return mine ? ((i & 1) ? (odd ? 0x03020c0cu : 0x01000c0cu) : (odd ? 0x0c0c0302u : 0x0c0c0100u)) : 0x0c0c0c0cu;
}
__device__ __forceinline__ unsigned get_tag(unsigned epoch) { PIN(epoch); return epoch + 1u; }
__device__ __forceinline__ unsigned umax4(v4u v) { return max(max(v.x, v.y), max(v.z, v.w)); }
__device__ __forceinline__ unsigned badtag(v4u v, unsigned tag) { return (v.y ^ tag) | (v.w ^ tag); }
__device__ __forceinline__ unsigned acctag(unsigned acc, v4u v, unsigned tag) {
    return __builtin_amdgcn_bitop3_b32(__builtin_amdgcn_bitop3_b32(acc, v.y, tag, 0xF6), v.w, tag, 0xF6);
}
__device__ __forceinline__ bool anyset(unsigned v) { return __builtin_amdgcn_ballot_w64(v != 0u) != 0ull; }

__global__ void __launch_bounds__(512, 2)
fused_mla_ffn(const u16* __restrict__ x, const u16* __restrict__ ckv, const u16* __restrict__ wq, const u16* __restrict__ wuk,
              const u16* __restrict__ wuv, const u16* __restrict__ wo, const u16* __restrict__ w1, const u16* __restrict__ b1,
              const u16* __restrict__ w2, const u16* __restrict__ b2, u16* __restrict__ out, unsigned char* ws) {
    __shared__ __attribute__((aligned(16))) unsigned char smem[LDS_BYTES];
    LAS unsigned char* L = (LAS unsigned char*)smem;
    STAMP(0);
    const unsigned long long tstart = __builtin_amdgcn_s_memrealtime();
    const int tid = threadIdx.x, lane = tid & 63, w = __builtin_amdgcn_readfirstlane(tid >> 6), i = lane & 15, g = lane >> 4;
    const int bid = blockIdx.x, xcd = bid & 7, idx = bid >> 3;
    unsigned codepf = 0;
    {
        unsigned long long pc;
        asm volatile("s_getpc_b64 %0" : "=s"(pc));
        const rsrc_t rsC = mkrs((const void*)(pc & ~127ull), CODE_LINES * 128);
        codepf = __builtin_amdgcn_raw_buffer_load_b32(rsC, (tid ^ 384) * 128, 0, 0);
    }
    SCHED();
    asm volatile("" ::"s"(x), "s"(ckv), "s"(wq), "s"(wuk), "s"(wuv), "s"(wo), "s"(w1), "s"(b1), "s"(w2), "s"(b2), "s"(out), "s"(ws));
    STAMP(11);
    const rsrc_t rsW = mkrs(ws, WS_BYTES);
    const rsrc_t rsX = mkrs(x, 2048);
    constexpr int S_UK = 98304;
    v4u xr0, xr1, xqa, xqb, a[16];
    unsigned ep1;
    if (idx < 16 && w < 2) {
        const int t8 = 2 * (idx & 3) + w, h1 = (4 * xcd + (idx >> 2)) ^ (2 + w);
        const rsrc_t rsQ = mkrs(wq, 512 * 8192);
#pragma unroll
        for (int u = 0; u < WQ_FIRST; ++u) a[u] = ld_nt(rsQ, g * 131072 + i * 16, (64 * t8 + u) * 8192 + h1 * 256);
        SCHED();
        xr0 = __builtin_amdgcn_raw_buffer_load_b128(rsX, i * 128 + g * 32, 0, 0);
        xr1 = __builtin_amdgcn_raw_buffer_load_b128(rsX, i * 128 + g * 32 + 16, 0, 0);
        xqa = __builtin_amdgcn_raw_buffer_load_b128(rsX, (i >> 3) * 1024 + g * 32, t8 * 128, 0);
        xqb = __builtin_amdgcn_raw_buffer_load_b128(rsX, (i >> 3) * 1024 + g * 32 + 16, t8 * 128, 0);
        ep1 = __builtin_amdgcn_raw_buffer_load_b32(rsW, WS_EPOCH, 0, AUX_SC1);
        SCHED();
#pragma unroll
        for (int u = WQ_FIRST; u < 16; ++u) a[u] = ld_nt(rsQ, g * 131072 + i * 16, (64 * t8 + u) * 8192 + h1 * 256);
        SCHED();
        STAMP(1);
    } else {
        __builtin_amdgcn_s_sleep(2);
        ep1 = __builtin_amdgcn_raw_buffer_load_b32(rsW, WS_EPOCH, 0, AUX_SC1);
    }
    unsigned epoch = ep1;
    if (idx < 16 && w >= 4) {
        if (UK_DELAY) __builtin_amdgcn_s_sleep(UK_DELAY);
        const unsigned char* base = (const unsigned char*)wuk + (size_t)((idx & 3) * 128 + 32 * (w - 4)) * 8192 + (4 * xcd + (idx >> 2)) * 256;
#pragma unroll
        for (int j = 0; j < 8; ++j) dma16(base + j * 32768, g * 8192 + ((i ^ ((4 * j + g) & 15)) * 16), S_UK + (w - 4) * 8192 + j * 1024);
        STAMP(1);
    }
    const v4u ones = {~0u, ~0u, ~0u, ~0u};
    const int q4 = (lane >> 2) & 3, p4 = lane & 3, trow = 4 * g + q4;

    if (__builtin_expect(idx >= 16, 0)) {
        const int hh = idx & 1, spa = xcd * 8 + ((idx - 16) >> 1), b = spa >> 5, aidx = spa * 2 + hh;
        const int tw = (w + 4 * hh) & 7;
        constexpr int QF = 1040, S_Q = 131072, S_P = S_Q + 16 * QF, S_M = S_P + 4096, S_L = S_M + 512;
        asm volatile("" ::"v"(codepf));
        PIN(epoch);
        while (__builtin_amdgcn_s_memrealtime() < tstart + A_START) __builtin_amdgcn_s_sleep(1);
        const int fidx = (aidx >> 1) + (aidx & 1) * 64;
        STAMP(1);
        {
            const unsigned char* base = (const unsigned char*)ckv + ((size_t)spa * 128 + 16 * tw) * 1024;
#pragma unroll
            for (int r = 0; r < 16; ++r) dma16(base + r * 1024, (lane ^ (((r & 7) << 1) | (r >> 3))) * 16, (16 * tw + r) * 1024);
        }
        st_dev(ones, rsW, tid * 16, WS_OB + aidx * 16384);
        st_dev(ones, rsW, tid * 16 + 8192, WS_OB + aidx * 16384);
        v4u kf[16], vf[4][4];
        if (w < 4) VMCNT(2);
        const unsigned tag = get_tag(epoch);
        asm volatile("" ::"v"(codepf));
        int vo[4];
        {
            const int X = ((trow & 7) << 1) | (trow >> 3);
#pragma unroll
            for (int T = 0; T < 4; ++T) { vo[T] = hh * 65536 + trow * 1024 + (w >> 1) * 256 + (((8 * (w & 1) + 2 * T + (p4 >> 1)) ^ X) * 16) + 8 * (p4 & 1); PIN(vo[T]); }
        }
        BARRIER();
        STAMP(17);
#pragma unroll
        for (int T = 0; T < 4; ++T)
#pragma unroll
            for (int s = 0; s < 2; ++s) vf[s][T] = tr_frag(L, vo[T] + s * 32768, vo[T] + s * 32768 + 16384);
#pragma unroll
        for (int T = 0; T < 4; ++T) { vo[T] ^= 65536; PIN(vo[T]); }
        int qw = S_Q + ((lane & 31) >> 1) * QF + ((lane >> 5) * 16 + 4 * w) * 16 + (lane & 1) * 8;
        int qr = S_Q + lane * 16, pw = S_P + (w >> 1) * 1024 + lane * 16 + (w & 1) * 8, mw = S_M + i * 32 + w * 4, mr = S_M + i * 32;
        int obo = (i & 7) * 128 + (i >> 3) * 64 + g * 16, ko = (16 * tw + i) * 1024 + g * 256 + ((((i & 7) << 1) | (i >> 3)) * 16);
        PIN(qw); PIN(qr); PIN(pw); PIN(mw); PIN(mr); PIN(obo); PIN(ko);
        if (w >= 4) VMCNT(2);
        STAMP(2);
#pragma unroll
        for (int s = 0; s < 16; ++s) kf[s] = *(LAS v4u*)(L + (ko ^ (s * 16)));
        if (w < 4) {
            v4u qg[8];
            const int qb = WS_QG + (b * 32 + hh * 16 + 4 * w) * 2048;
            while (__builtin_amdgcn_s_memrealtime() < tstart + E2_START) __builtin_amdgcn_s_sleep(1);
#pragma unroll
            for (int t = 0; t < 8; ++t) qg[t] = ld_dev(rsW, lane * 16 + (t & 3) * 1024, qb + (t >> 2) * 4096);
            SCHED();
            unsigned bad = 0u;
#pragma unroll
            for (int t = 0; t < 8; ++t) {
                bad = acctag(bad, qg[t], tag);
                *(LAS unsigned*)(L + qw + (t & 1) * 512 + (t >> 1) * 16) = qg[t].x;
                *(LAS unsigned*)(L + qw + (t & 1) * 512 + (t >> 1) * 16 + 4) = qg[t].z;
                SCHED();
            }
            if (__builtin_expect(anyset(bad), 0)) {
                for (unsigned it = 0; it < SPIN_CAP; ++it) {
                    asm volatile("; retry qabs" ::: "memory");
                    __builtin_amdgcn_s_sleep(E2_SLEEP);
#pragma unroll
                    for (int t = 0; t < 8; ++t)
                        if (anyset(badtag(qg[t], tag))) qg[t] = ld_dev(rsW, lane * 16 + (t & 3) * 1024, qb + (t >> 2) * 4096);
                    bad = 0u;
#pragma unroll
                    for (int t = 0; t < 8; ++t) bad = acctag(bad, qg[t], tag);
                    if (!anyset(bad)) break;
                }
#pragma unroll
                for (int t = 0; t < 8; ++t) *(LAS v2u*)(L + qw + (t & 1) * 512 + (t >> 1) * 16) = v2u{qg[t].x, qg[t].z};
            }
        }
        STAMP(4);
        CSTAMP(12);
        BARRIER();
        CSTAMP(13);
        STAMP(3);
        float Mx;
        {
            f32x4 d = {0.f, 0.f, 0.f, 0.f};
            v4u qf[16];
#pragma unroll
            for (int s = 0; s < 16; ++s) qf[s] = *(LAS v4u*)(L + qr + s * QF);
            SCHED();
#pragma unroll
            for (int s = 0; s < 16; ++s) {
                d = mfma(kf[s], qf[s], d);
                if (s >= 8) { const int T = (s - 8) >> 1, ss = s & 1; vf[2 + ss][T] = tr_frag(L, vo[T] + ss * 32768, vo[T] + ss * 32768 + 16384); }
                SCHED();
            }
            const float SC = 0.08838834764831845f, LOG2E = 1.4426950408889634f;
            const unsigned r0 = pk_bf16(d.x, d.y), r1 = pk_bf16(d.z, d.w);
            const unsigned s0 = pk_bf16(bf_lo(r0) * SC, bf_hi(r0) * SC), s1 = pk_bf16(bf_lo(r1) * SC, bf_hi(r1) * SC);
            const float t0 = bf_lo(s0), t1 = bf_hi(s0), t2 = bf_lo(s1), t3 = bf_hi(s1);
            const float mt = max_x32(max_x16(vmax(vmax3(t0, t1, t2), t3)));
            if (g == 0) *(LAS float*)(L + mw) = mt;
            CSTAMP(14);
            BARRIER();
            const f32x4 ma = *(LAS f32x4*)(L + mr), mb = *(LAS f32x4*)(L + mr + 16);
            Mx = __builtin_ceilf(vmax3(vmax3(ma.x, ma.y, ma.z), vmax3(ma.w, mb.x, mb.y), vmax(mb.z, mb.w)) * LOG2E);
            const float p0 = __builtin_amdgcn_exp2f(fmaf(t0, LOG2E, -Mx)), p1 = __builtin_amdgcn_exp2f(fmaf(t1, LOG2E, -Mx));
            const float p2 = __builtin_amdgcn_exp2f(fmaf(t2, LOG2E, -Mx)), p3 = __builtin_amdgcn_exp2f(fmaf(t3, LOG2E, -Mx));
            *(LAS v2u*)(L + pw) = v2u{pk_bf16(p0, p1), pk_bf16(p2, p3)};
            *(LAS float*)(L + S_L + w * 256 + lane * 4) = (p0 + p1) + (p2 + p3);
        }
        VMCNT(0);
        CSTAMP(15);
        BARRIER();
        STAMP(5);
        CSTAMP(16);
        {
            v4u pf[4];
#pragma unroll
            for (int s = 0; s < 4; ++s) pf[s] = *(LAS v4u*)(L + qr + (S_P - S_Q) + s * 1024);
            if (w == 4) {
                float ls = 0.f;
#pragma unroll
                for (int t = 0; t < 8; ++t) ls += *(LAS float*)(L + S_L + t * 256 + lane * 4);
                ls = sum_x32(sum_x16(ls));
                if (g == 0) st_dev(v4u{fbits(Mx), tag, fbits(ls), tag}, rsW, lane * 16, WS_ST + aidx * 256);
            }
            SCHED();
            f32x4 d[4];
#pragma unroll
            for (int T = 0; T < 4; ++T) d[T] = f32x4{0.f, 0.f, 0.f, 0.f};
#pragma unroll
            for (int s = 0; s < 4; ++s)
#pragma unroll
                for (int T = 0; T < 4; ++T) d[T] = mfma(vf[s][T], pf[s], d[T]);
            const v4u t0 = {nosent(pk_bf16(d[0].x, d[0].y)), nosent(pk_bf16(d[0].z, d[0].w)), nosent(pk_bf16(d[1].x, d[1].y)), nosent(pk_bf16(d[1].z, d[1].w))};
            const v4u t1 = {nosent(pk_bf16(d[2].x, d[2].y)), nosent(pk_bf16(d[2].z, d[2].w)), nosent(pk_bf16(d[3].x, d[3].y)), nosent(pk_bf16(d[3].z, d[3].w))};
            v4u sa, sb;
#pragma unroll
            for (int k = 0; k < 4; ++k) {
                sa[k] = (unsigned)__builtin_amdgcn_update_dpp((int)t0[k], (int)t1[k], DPP_ROR8, 0xf, 0xc, false);
                sb[k] = (unsigned)__builtin_amdgcn_update_dpp((int)t1[k], (int)t0[k], DPP_ROR8, 0xf, 0x3, false);
            }
            st_dev(sa, rsW, obo, WS_OB + aidx * 16384 + w * 2048);
            st_dev(sb, rsW, obo + 1024, WS_OB + aidx * 16384 + w * 2048);
        }
        STAMP(6);
        CSTAMP(18);
        const unsigned long long tpub = __builtin_amdgcn_s_memrealtime();
        if (fidx < NFFN) {
            constexpr int S_H = 16384;
            __builtin_amdgcn_s_sleep(W2_DELAY);
            {
                const unsigned char* b2w = (const unsigned char*)w2 + (size_t)fidx * 16384 + w * 2048;
                dma16(b2w, lane * 16, w * 2048);
                dma16(b2w + 1024, lane * 16, w * 2048 + 1024);
            }
            if (w == 0) {
                const unsigned bias = *(const unsigned*)(b1 + fidx * 16 + (lane & 1) * 8 + ((lane >> 1) & 1) * 4 + ((lane >> 3) & 1) * 2);
                const int ho = (lane >> 3) * 2048 + (((lane >> 1) & 3) * 16 + (lane & 1)) * 16, hs = WS_H1 + (fidx >> 3) * 16384 + (fidx & 7) * 32;
                v4u o0, o1;
                for (unsigned it = 0;; ++it) {
                    o0 = ld_dev(rsW, ho, hs);
                    o1 = ld_dev(rsW, ho + 1024, hs);
                    if (__all(((o0.y ^ tag) | (o0.w ^ tag) | (o1.y ^ tag) | (o1.w ^ tag)) == 0u) || it > SPIN_CAP) break;
                    asm volatile("" ::: "memory");
                }
                float v[4] = {bitsf(o0.x), bitsf(o0.z), bitsf(o1.x), bitsf(o1.z)};
#pragma unroll
                for (int e = 0; e < 4; ++e) { v[e] += dpp<DPP_ROR8>(v[e]); v[e] = sum_x32(sum_x16(v[e])); }
                float hq[2];
                const float va = (lane & 8) ? v[2] : v[0], vb = (lane & 8) ? v[3] : v[1];
#pragma unroll
                for (int e = 0; e < 2; ++e) {
                    const float hv = rbf(rbf(e ? vb : va) + (e ? bf_hi(bias) : bf_lo(bias)));
                    const float u2 = 2.0f * 0.7978845608028654f * 1.4426950408889634f * (hv + 0.044715f * hv * hv * hv);
                    const float th = 1.0f - 2.0f / (__builtin_amdgcn_exp2f(u2) + 1.0f);
                    hq[e] = 0.5f * hv * (1.0f + th);
                }
                if (lane < 16) *(LAS unsigned*)(L + S_H + ((lane >> 2) & 1) * 32 + ((lane & 1) * 8 + ((lane >> 1) & 1) * 4 + ((lane >> 3) & 1) * 2) * 2) = pk_bf16(hq[0], hq[1]);
            }
            VMCNT(0);
            BARRIER();
            if (w < 4) {
                const v2u hp = *(LAS v2u*)(L + S_H + (i >> 3) * 32 + g * 8);
                f32x4 y = {0.f, 0.f, 0.f, 0.f};
#pragma unroll
                for (int u = 0; u < 4; ++u) {
                    v4u af;
#pragma unroll
                    for (int k = 0; k < 4; ++k) af[k] = __builtin_amdgcn_perm(hp[u >> 1], hp[u >> 1], sel_of(i, k, u & 1));
                    y = mfma(af, *(LAS v4u*)(L + (4 * g + u) * 1024 + w * 256 + i * 16), y);
                }
                const int yo = ((g >> 1) * 32 + (i >> 1)) * R5_CONS + ((i & 1) * 4 + (g & 1) * 2) * 16, ys = WS_R5 + 8 * w * R5_CONS + (128 + fidx) * 128;
                st_dev(v4u{fbits(y.x), tag, fbits(y.y), tag}, rsW, yo, ys);
                st_dev(v4u{fbits(y.z), tag, fbits(y.w), tag}, rsW, yo + 16, ys);
            }
            STAMP(7);
        }
        if (aidx & 1) {
            const int c64 = aidx >> 1, rb = WS_R5 + c64 * R5_CONS;
            constexpr int S_RY = 32768, S_RS = S_RY + 512;
            const unsigned xo = x[c64 * 16 + i], bo = b2[(c64 & 31) * 16 + i];
            const bool has2 = 64 + 8 * w + (lane >> 3) < NFFN;
            while (__builtin_amdgcn_s_memrealtime() < tpub + FINY_START) __builtin_amdgcn_s_sleep(2);
            {
                v4u y0, y1;
                for (unsigned it = 0;; ++it) {
                    y0 = ld_dev(rsW, lane * 16, rb + 16384 + w * 1024);
                    y1 = ld_dev(rsW, (has2 ? 8192 : 0) + lane * 16, rb + 16384 + w * 1024);
                    if (__all((badtag(y0, tag) | badtag(y1, tag)) == 0u) || it > SPIN_CAP) break;
                    __builtin_amdgcn_s_sleep(2);
                    asm volatile("" ::: "memory");
                }
                float v[2] = {bitsf(y0.x) + (has2 ? bitsf(y1.x) : 0.f), bitsf(y0.z) + (has2 ? bitsf(y1.z) : 0.f)};
#pragma unroll
                for (int e = 0; e < 2; ++e) { v[e] += dpp<DPP_ROR8>(v[e]); v[e] = sum_x32(sum_x16(v[e])); }
                if (lane < 8) *(LAS f32x2*)(L + S_RY + w * 64 + lane * 8) = f32x2{v[0], v[1]};
            }
            while (__builtin_amdgcn_s_memrealtime() < tpub + FIN_START) __builtin_amdgcn_s_sleep(1);
            STAMP(8);
            v4u r0 = ld_dev(rsW, lane * 16, rb + w * 2048), r1 = ld_dev(rsW, lane * 16 + 1024, rb + w * 2048);
            SCHED();
            BARRIER();
            int rsw = S_RS + w * 64 + (lane & 7) * 8;
            PIN(rsw);
            float gt;
            {
                const float ys = sum_x32(sum_x16(*(LAS float*)(L + S_RY + g * 128 + i * 4) + *(LAS float*)(L + S_RY + g * 128 + 64 + i * 4)));
                const float y = rbf(rbf(ys) + bf_lo(bo));
                gt = rbf(y * __builtin_amdgcn_rcpf(1.0f + __builtin_amdgcn_exp2f(-1.4426950408889634f * y)));
                PIN(gt);
            }
            SCHED();
            {
                auto reduce = [&]() {
                    float v[2] = {bitsf(r0.x) + bitsf(r1.x), bitsf(r0.z) + bitsf(r1.z)};
#pragma unroll
                    for (int e = 0; e < 2; ++e) { v[e] += dpp<DPP_ROR8>(v[e]); v[e] = sum_x32(sum_x16(v[e])); }
                    *(LAS f32x2*)(L + rsw) = f32x2{v[0], v[1]};
                };
                const unsigned bad = acctag(acctag(0u, r0, tag), r1, tag);
                reduce();
                if (__builtin_expect(anyset(bad), 0)) {
                    for (unsigned it = 0; it < SPIN_CAP; ++it) {
                        asm volatile("; retry out" ::: "memory");
                        if (anyset(badtag(r0, tag))) r0 = ld_dev(rsW, lane * 16, rb + w * 2048);
                        if (anyset(badtag(r1, tag))) r1 = ld_dev(rsW, lane * 16 + 1024, rb + w * 2048);
                        if (!anyset(acctag(acctag(0u, r0, tag), r1, tag))) break;
                    }
                    reduce();
                }
            }
            STAMP(9);
            BARRIER();
            if (w == 0) {
                const float s = sum_x32(sum_x16(*(LAS float*)(L + S_RS + g * 128 + i * 4) + *(LAS float*)(L + S_RS + g * 128 + 64 + i * 4)));
                if (g == 0) out[c64 * 16 + i] = (u16)pk_bf16(bf_lo(xo) + rbf(rbf(s) * gt), 0.f);
                if (c64 == 0 && lane == 0) __builtin_amdgcn_raw_buffer_store_b32(tag, rsW, WS_EPOCH, 0, AUX_SC1);
            }
            STAMP(10);
        }
        FINISH();
        return;
    }
    {
        const int h = 4 * xcd + (idx >> 2), cq = idx & 3, fb = h * 4 + cq;
        constexpr int S_WO = 0, S_UV = 65536, S_QB = 131072;
        v4u uk[2][4], af[16];
        int qro, pubo;
        unsigned tag;
        const int t8 = 2 * cq + (w & 1);
        if (__builtin_expect(w < 2, 1)) {
            const int h1 = h ^ (2 + w);
            unsigned sl[2][4];
#pragma unroll
            for (int k = 0; k < 4; ++k) { sl[0][k] = sel_of(i, k, 0); sl[1][k] = sel_of(i, k, 1); PIN(sl[0][k]); PIN(sl[1][k]); }
            SCHED();
            float mean, sd;
            ln_stats(xr0, xr1, i, mean, sd);
            const float rc = __builtin_amdgcn_rcpf(sd);
            STAMP(7);
            f32x4 acc = {0.f, 0.f, 0.f, 0.f};
#pragma unroll
            for (int e = 0; e < 8; ++e) {
                const unsigned xp = ln_apply(e < 4 ? xqa[e & 3] : xqb[e & 3], mean, sd, rc);
#pragma unroll
                for (int k = 0; k < 4; ++k) { af[2 * e][k] = __builtin_amdgcn_perm(xp, xp, sl[0][k]); af[2 * e + 1][k] = __builtin_amdgcn_perm(xp, xp, sl[1][k]); }
                if (e >= LAG) { acc = mfma(af[2 * (e - LAG)], a[2 * (e - LAG)], acc); acc = mfma(af[2 * (e - LAG) + 1], a[2 * (e - LAG) + 1], acc); }
                SCHED();
            }
#pragma unroll
            for (int u = 16 - 2 * LAG; u < 16; ++u) acc = mfma(af[u], a[u], acc);
            SCHED();
            const unsigned tg = get_tag(ep1);
            tag = tg;
            asm volatile("" ::"v"(codepf));
            st_dev(v4u{fbits(acc.x), tg, fbits(acc.y), tg}, rsW, lane * 16, WS_Q1 + (h1 * 8 + t8) * 2048);
            st_dev(v4u{fbits(acc.z), tg, fbits(acc.w), tg}, rsW, lane * 16 + 1024, WS_Q1 + (h1 * 8 + t8) * 2048);
            STAMP(19);
            int qo = S_QB + (g >> 1) * 256 + (8 * i + 4 * (g & 1) + 2 * w) * 2;
            PIN(qo);
            if (E1_DELAY) __builtin_amdgcn_s_sleep(E1_DELAY); else VMCNT(0);
            STAMP(17);
            v4u o[8];
#pragma unroll
            for (int k = 0; k < 8; ++k) o[k] = ld_dev(rsW, lane * 16 + (k & 1) * 2048, WS_Q1 + h * 16384 + w * 1024 + (k >> 1) * 4096);
            SCHED();
            {
                unsigned bad = 0u;
                float sx = 0.f, sz = 0.f;
#pragma unroll
                for (int k = 0; k < 8; ++k) { bad = acctag(bad, o[k], tg); sx += bitsf(o[k].x); sz += bitsf(o[k].z); PIN(sx); PIN(sz); }
                *(LAS unsigned*)(L + qo) = pk_bf16(sx, sz);
                if (__builtin_expect(anyset(bad), 0)) {
                    for (unsigned it = 0; it < SPIN_CAP; ++it) {
                        asm volatile("; retry q" ::: "memory");
#pragma unroll
                        for (int k = 0; k < 8; ++k)
                            if (anyset(badtag(o[k], tg))) o[k] = ld_dev(rsW, lane * 16 + (k & 1) * 2048, WS_Q1 + h * 16384 + w * 1024 + (k >> 1) * 4096);
                        bad = 0u;
#pragma unroll
                        for (int k = 0; k < 8; ++k) bad = acctag(bad, o[k], tg);
                        if (!anyset(bad)) break;
                    }
                    sx = 0.f; sz = 0.f;
#pragma unroll
                    for (int k = 0; k < 8; ++k) { sx += bitsf(o[k].x); sz += bitsf(o[k].z); }
                    *(LAS unsigned*)(L + qo) = pk_bf16(sx, sz);
                }
            }
            STAMP(9);
        } else if (w >= 4) {
            VMCNT(0);
#pragma unroll
            for (int t = 0; t < 2; ++t)
#pragma unroll
                for (int s = 0; s < 4; ++s) uk[t][s] = *(LAS v4u*)(L + S_UK + (w - 4) * 8192 + (16 * t + i) * 256 + (((4 * s + g) ^ i) * 16));
            tag = get_tag(epoch);
            asm volatile("" ::"v"(codepf));
            qro = S_QB + (i & 1) * 256 + g * 16; pubo = (i & 1) * 65536 + ((i >> 1) * 4 + g) * 16;
            PIN(qro); PIN(pubo); PIN(tag);
            STAMP(8);
        } else {
            tag = get_tag(epoch);
            asm volatile("" ::"v"(codepf));
        }
        BARRIER();
        if (w >= 4) {
            v4u qf[4];
#pragma unroll
            for (int s = 0; s < 4; ++s) qf[s] = *(LAS v4u*)(L + qro + s * 64);
            SCHED();
            f32x4 d0 = {0.f, 0.f, 0.f, 0.f}, d1 = {0.f, 0.f, 0.f, 0.f};
#pragma unroll
            for (int s = 0; s < 4; ++s) { d0 = mfma(uk[0][s], qf[s], d0); d1 = mfma(uk[1][s], qf[s], d1); }
            const f32x4 d = (i & 2) ? d1 : d0;
            if (i < 4)
                st_dev(v4u{pk_bf16(d.x, d.y), tag, pk_bf16(d.z, d.w), tag}, rsW, pubo, WS_QG + h * 2048 + cq * 512 + (w - 4) * 128);
        }
        STAMP(3);
        SCHED();
        __builtin_amdgcn_s_barrier();
        SCHED();
        const int ch = cq >> 1, dh = cq & 1, slab = (w + 4 * ch) & 7;
        int r5o = ((i & 1) * 32 + (i >> 2)) * R5_CONS + (2 * g + ((i >> 1) & 1)) * 16;
        const int r5s = WS_R5 + 4 * slab * R5_CONS + fb * 128;
        if (B_DELAY) __builtin_amdgcn_s_sleep(B_DELAY);
        const int nh = (h >> 2) * 3 + ((h & 3) ? (h & 3) - 1 : 0), cb = nh >> 1;
        const bool ffn1 = (h & 3) != 1 && cb < 11 && w == (nh & 1);
        if (ffn1) {
            const unsigned char* base = (const unsigned char*)w1 + (size_t)(64 * t8) * 2752 + cb * 256;
            const unsigned vo = g * (16 * 2752) + (cb * 256 + i * 16 < 2752 ? i * 16 : 0);
#pragma unroll
            for (int u = 0; u < 16; ++u) dma16(base + u * 2752, vo, S_UK + u * 1024);
        }
        {
            const int r8 = lane >> 3;
            const unsigned sw = ((lane & 7) ^ (((r8 >> 1) & 3) << 1)) * 16;
            const unsigned char* base = (const unsigned char*)wuv + (size_t)(ch * 256 + 32 * w) * 8192 + h * 256 + dh * 128;
#pragma unroll
            for (int j = 0; j < 4; ++j) dma16(base + j * 65536, r8 * 8192 + sw, S_UV + (32 * w + 8 * j) * 128);
            const unsigned char* bo = (const unsigned char*)wo + (size_t)h * 131072 + dh * 65536 + slab * 128;
#pragma unroll
            for (int j = 0; j < 8; ++j) dma16(bo + j * 8192, r8 * 1024 + sw, S_WO + w * 8192 + j * 1024);
        }
        if (ffn1) {
            VMCNT(12);
            f32x4 acc = {0.f, 0.f, 0.f, 0.f};
#pragma unroll
            for (int u = 0; u < 16; ++u) acc = mfma(af[u], *(LAS v4u*)(L + S_UK + u * 1024 + lane * 16), acc);
            st_dev(v4u{fbits(acc.x), tag, fbits(acc.y), tag}, rsW, lane * 16, WS_H1 + (cb * 8 + t8) * 2048);
            st_dev(v4u{fbits(acc.z), tag, fbits(acc.w), tag}, rsW, lane * 16 + 1024, WS_H1 + (cb * 8 + t8) * 2048);
            STAMP(10);
            VMCNT(2);
        } else {
            VMCNT(0);
        }
        v4u fuv[4], fo[2][4];
        const int tsw = ((trow >> 1) & 3) << 1;
#pragma unroll
        for (int s = 0; s < 2; ++s)
#pragma unroll
            for (int T = 0; T < 4; ++T) {
                const int o = S_WO + w * 8192 + (s * 32 + trow) * 128 + (((2 * T + (p4 >> 1)) ^ tsw) * 16) + 8 * (p4 & 1);
                fo[s][T] = tr_frag(L, o, o + 16 * 128);
            }
        BARRIER();
#pragma unroll
        for (int s = 0; s < 4; ++s) {
            const int o = S_UV + (128 * (w >> 2) + 32 * s + trow) * 128 + (((2 * (w & 3) + (p4 >> 1)) ^ tsw) * 16) + 8 * (p4 & 1);
            fuv[s] = tr_frag(L, o, o + 16 * 128);
        }
        constexpr int CC_ROW = 272, S_CC = 136192, S_CTX = S_CC + 32 * CC_ROW, S_WG = S_CTX + 512, S_RL = S_WG + 256;
        int ccw = S_CC + (8 * (g >> 1) + w) * CC_ROW + (8 * i + 4 * (g & 1)) * 2;
        int ccr = S_CC + ((w >> 2) * 16 + i) * CC_ROW + g * 16;
        int cxw = S_CTX + ((g >> 1) + 2 * (w >> 2)) * 128 + (32 * ((w >> 1) & 1) + 8 * (i >> 2) + 4 * (w & 1) + (i & 3)) * 2;
        int cxr = S_CTX + (i & 3) * 128 + g * 16;
        int po = g * 32768 + (i >> 3) * 2048 + (i & 7) * 16;
        const int ps = WS_OB + (8 * w + (h >> 4)) * 16384 + ch * 8192 + (h & 15) * 128;
        PIN(ccw); PIN(ccr); PIN(cxw); PIN(cxr); PIN(r5o); PIN(po);
        STAMP(4);
        if (w == 7) {
            v4u ml;
            for (unsigned it = 0;; ++it) {
                ml = ld_dev(rsW, i * 2048 + g * 512, WS_ST + (h >> 4) * 256 + (h & 15) * 16);
                if (__all(badtag(ml, tag) == 0u) || it > SPIN_CAP) break;
                __builtin_amdgcn_s_sleep(ST_SLEEP);
                asm volatile("" ::: "memory");
            }
            const float Mx = max_x32(max_x16(max8(bitsf(ml.x))));
            const float wgt = __builtin_amdgcn_exp2f(bitsf(ml.x) - Mx);
            *(LAS float*)(L + S_WG + lane * 4) = wgt;
            const float rl = __builtin_amdgcn_rcpf(sum_x32(sum_x16(sum8(wgt * bitsf(ml.z)))));
            if ((lane & 55) == 0) *(LAS float*)(L + S_RL + (lane >> 1)) = rl;
        }
        BARRIER();
        STAMP(2);
        v4u pr[2][2], a0, a1;
        float rL;
        {
            pr[0][0] = ld_dev(rsW, po, ps);
            pr[1][0] = ld_dev(rsW, po + 64 * 16384, ps);
            pr[0][1] = ld_dev(rsW, po, ps + 4096);
            pr[1][1] = ld_dev(rsW, po + 64 * 16384, ps + 4096);
            SCHED();
            const unsigned wg = *(LAS unsigned*)(L + S_WG + (16 * g + (i & 8) + w) * 4);
            rL = *(LAS float*)(L + S_RL + (g >> 1) * 4);
#pragma unroll
            for (int k = 0; k < 4; ++k) {
                a0[k] = __builtin_amdgcn_perm(wg, wg, i < 8 ? sel_of(i, k, 1) : 0x0c0c0c0cu);
                a1[k] = __builtin_amdgcn_perm(wg, wg, i < 8 ? 0x0c0c0c0cu : sel_of(i, k, 1));
            }
            PIN(a0); PIN(a1); PIN(rL);
            SCHED();
        }
        {
            auto combine = [&]() {
                f32x4 d0 = {0.f, 0.f, 0.f, 0.f}, d1 = {0.f, 0.f, 0.f, 0.f};
                d0 = mfma(a0, pr[0][0], d0); d0 = mfma(a1, pr[1][0], d0);
                d1 = mfma(a0, pr[0][1], d1); d1 = mfma(a1, pr[1][1], d1);
                *(LAS v2u*)(L + ccw) = v2u{pk_bf16(d0.x, d0.y), pk_bf16(d0.z, d0.w)};
                *(LAS v2u*)(L + ccw + 16 * CC_ROW) = v2u{pk_bf16(d1.x, d1.y), pk_bf16(d1.z, d1.w)};
            };
            const unsigned mx = max(max(umax4(pr[0][0]), umax4(pr[1][0])), max(umax4(pr[0][1]), umax4(pr[1][1])));
            combine();
            if (__builtin_expect(__builtin_amdgcn_ballot_w64(mx == ~0u) != 0ull, 0)) {
                for (unsigned it = 0; it < SPIN_CAP; ++it) {
                    asm volatile("; retry partials" ::: "memory");
                    if (__any(umax4(pr[0][0]) == ~0u)) pr[0][0] = ld_dev(rsW, po, ps);
                    if (__any(umax4(pr[1][0]) == ~0u)) pr[1][0] = ld_dev(rsW, po + 64 * 16384, ps);
                    if (__any(umax4(pr[0][1]) == ~0u)) pr[0][1] = ld_dev(rsW, po, ps + 4096);
                    if (__any(umax4(pr[1][1]) == ~0u)) pr[1][1] = ld_dev(rsW, po + 64 * 16384, ps + 4096);
                    if (__all(max(max(umax4(pr[0][0]), umax4(pr[1][0])), max(umax4(pr[0][1]), umax4(pr[1][1]))) != ~0u)) break;
                }
                combine();
            }
        }
        STAMP(5);
        CSTAMP(12);
        CSTAMP(13);
        BARRIER();
        CSTAMP(14);
        {
            v4u cf[4];
#pragma unroll
            for (int s = 0; s < 4; ++s) cf[s] = *(LAS v4u*)(L + ccr + s * 64);
            SCHED();
            f32x4 d = {0.f, 0.f, 0.f, 0.f};
#pragma unroll
            for (int s = 0; s < 4; ++s) d = mfma(cf[s], fuv[s], d);
            const float c = sum_x16((d.x + d.y) + (d.z + d.w)) * rL;
            if (!(g & 1)) *(LAS u16*)(L + cxw) = (u16)pk_bf16(c, c);
        }
        CSTAMP(15);
        BARRIER();
        CSTAMP(16);
        {
            const v4u c0 = *(LAS v4u*)(L + cxr), c1 = *(LAS v4u*)(L + cxr + 64);
            f32x4 d[4];
#pragma unroll
            for (int T = 0; T < 4; ++T) d[T] = mfma(fo[0][T], c0, f32x4{0.f, 0.f, 0.f, 0.f});
#pragma unroll
            for (int T = 0; T < 4; ++T) d[T] = mfma(fo[1][T], c1, d[T]);
            const f32x4 e0 = (i & 4) ? d[1] : d[0], e1 = (i & 4) ? d[3] : d[2];
            f32x4 e = (i & 8) ? e1 : e0;
            e.x += dpp<DPP_X2>(e.x); e.y += dpp<DPP_X2>(e.y); e.z += dpp<DPP_X2>(e.z); e.w += dpp<DPP_X2>(e.w);
            st_dev(v4u{fbits((i & 2) ? e.z : e.x), tag, fbits((i & 2) ? e.w : e.y), tag}, rsW, r5o, r5s);
        }
        STAMP(6);
        CSTAMP(18);
        FINISH();
        __builtin_amdgcn_s_waitcnt(0);
    }
}

extern "C" void kernel_launch(void* const* d_in, const int* in_sizes, int n_in, void* d_out, int out_size, void* d_ws, size_t ws_size,
                              hipStream_t stream) {
    fused_mla_ffn<<<256, 512, 0, stream>>>((const u16*)d_in[0], (const u16*)d_in[1], (const u16*)d_in[2], (const u16*)d_in[3], (const u16*)d_in[4],
                                           (const u16*)d_in[5], (const u16*)d_in[7], (const u16*)d_in[8], (const u16*)d_in[9], (const u16*)d_in[10],
                                           (u16*)d_out, (unsigned char*)d_ws + WS_BASE);
}
```
